```python
import torch

def _softmax(x, axis=-1):
    return torch.softmax(x, dim=axis)
def _gelu(x):
    return torch.nn.functional.gelu(x, approximate="tanh")
def _silu(x):
    return torch.nn.functional.silu(x)
def _sigmoid(x):
    return torch.sigmoid(x)
def _layernorm(x, axis=-1, eps=1e-5):
    m = x.mean(dim=axis, keepdim=True)
    v = x.var(dim=axis, keepdim=True, unbiased=False)
    return (x - m) / torch.sqrt(v + eps)
def _scatter_add(tgt, idx, src, axis=0):
    return tgt.clone().index_add_(axis, idx, src)
def _segment_sum(src, seg, n):
    out = torch.zeros((n,) + src.shape[1:], dtype=src.dtype)
    return out.index_add_(0, seg, src)
def _pad(x, pw):
    flat = tuple(v for pair in reversed(pw) for v in pair)
    return torch.nn.functional.pad(x, flat)

kernel_name = 'sequential_preLN__gqa_block_cached__glu_gated_reglu__decode'

def setup_inputs():

    _w = (lambda a: (lambda t: t.to(torch.bfloat16) if t.is_floating_point() else t)(torch.as_tensor(a)))
    def _A_setup():
        B, Sq, D = 1, 1, 512
        Hq, Hkv, Sk, Dh = 32, 8, 65536, 128
        assert Hq % Hkv == 0, (Hq, Hkv)
        return {
            "x": _w((lambda *s: torch.randn(*s, dtype=torch.bfloat16) * 0.05)(B, Sq, D)),
            "k_cache": _w((lambda *s: torch.randn(*s, dtype=torch.bfloat16) * 3.2)(B, Hkv, Sk, Dh)),
            "v_cache": _w((lambda *s: torch.randn(*s, dtype=torch.bfloat16) * 1.6)(B, Hkv, Sk, Dh)),
            "wq": _w((lambda *s: torch.randn(*s, dtype=torch.bfloat16) * 3.2)(D, Hq * Dh) * (D ** -0.5)),
            "wo": _w((lambda *s: torch.randn(*s, dtype=torch.bfloat16) * 1.6)(Hq * Dh, D) * ((Hq * Dh) ** -0.5)),
        }
    _A_in = _A_setup()

    def _B_setup():
        B, S, D, F = 1, 1, 512, 1792
        return {
            "x": _w((lambda *s: torch.randn(*s, dtype=torch.bfloat16) * 0.05)(B, S, D)),
            "W_gate": _w((lambda *s: torch.randn(*s, dtype=torch.bfloat16) * 1.6)(D, F) * (2.0 / D) ** 0.5),
            "W_up": _w((lambda *s: torch.randn(*s, dtype=torch.bfloat16) * 1.6)(D, F) * (2.0 / D) ** 0.5),
            "W_down": _w((lambda *s: torch.randn(*s, dtype=torch.bfloat16) * 1.6)(F, D) * (1.0 / F) ** 0.5),
        }
    _B_in = _B_setup()

    return {
        "A_x": _A_in["x"],
        "A_k_cache": _A_in["k_cache"],
        "A_v_cache": _A_in["v_cache"],
        "A_wq": _A_in["wq"],
        "A_wo": _A_in["wo"],
        "B_x": _B_in["x"],
        "B_W_gate": _B_in["W_gate"],
        "B_W_up": _B_in["W_up"],
        "B_W_down": _B_in["W_down"],
    }

def reference(A_x, A_k_cache, A_v_cache, A_wq, A_wo, B_x, B_W_gate, B_W_up, B_W_down):
    def _A_ref(x, k_cache, v_cache, wq, wo):
        B, Sq, D = x.shape
        Hq, Dh = 32, 128
        Hkv = k_cache.shape[1]
        rep = Hq // Hkv
        q = (lambda x, *d: x.permute(*d))((x @ wq).reshape(B, Sq, Hq, Dh), 0, 2, 1, 3)
        k = (lambda x, n, axis: torch.repeat_interleave(x, n, dim=axis))(k_cache, rep, axis=1)
        v = (lambda x, n, axis: torch.repeat_interleave(x, n, dim=axis))(v_cache, rep, axis=1)
        scores = torch.einsum("bhqd,bhkd->bhqk", q, k) * (Dh ** -0.5)
        p = _softmax(scores, axis=-1)
        o = torch.einsum("bhqk,bhkd->bhqd", p, v)
        return (lambda x, *d: x.permute(*d))(o, 0, 2, 1, 3).reshape(B, Sq, Hq * Dh) @ wo

    def _B_ref(x, W_gate, W_up, W_down):
        gate = torch.einsum("bsd,df->bsf", x, W_gate)
        up = torch.einsum("bsd,df->bsf", x, W_up)
        gate = gate * (lambda x, dt: x.to(dt))(gate > 0, gate.dtype)
        h = gate * up
        return torch.einsum("bsf,fd->bsd", h, W_down)

    x = A_x
    h = x + _A_ref(_layernorm(x, axis=-1), A_k_cache, A_v_cache, A_wq, A_wo)
    h2 = h + _B_ref(_layernorm(h, axis=-1), B_W_gate, B_W_up, B_W_down)
    return h2
```

```cpp
#include <hip/hip_runtime.h>
#include <hip/hip_bf16.h>
#include <cstdint>

typedef unsigned short bf16_t;
using bf16x2 = __attribute__((ext_vector_type(2))) __bf16;
using u32x4  = __attribute__((ext_vector_type(4))) unsigned;
using f32x4  = __attribute__((ext_vector_type(4))) float;

constexpr int D = 512, HQ = 32, HKV = 8, SK = 65536, DH = 128, F = 1792;
constexpr int NSPLIT = 64;
constexpr int WAVES = 4;

__device__ __forceinline__ float bf2f(unsigned short b) { return __builtin_bit_cast(float, (unsigned)b << 16); }
__device__ __forceinline__ unsigned short f2bf(float f) { return __builtin_bit_cast(unsigned short, (__bf16)f); }
__device__ __forceinline__ float rbf(float f) { return bf2f(f2bf(f)); }
__device__ __forceinline__ float dot2(unsigned k, unsigned q, float acc) {
  return __builtin_amdgcn_fdot2_f32_bf16(__builtin_bit_cast(bf16x2, k), __builtin_bit_cast(bf16x2, q), acc, false);
}
template <int CTRL> __device__ __forceinline__ float dpp(float x) {
  return __builtin_bit_cast(float, __builtin_amdgcn_mov_dpp(__builtin_bit_cast(int, x), CTRL, 0xf, 0xf, true));
}
constexpr int XOR1 = 0xB1, XOR2 = 0x4E, XOR7 = 0x141, XOR8 = 0x128, ROR4 = 0x124;
__device__ __forceinline__ float xrow16_max(float x) {
  auto s = __builtin_amdgcn_permlane16_swap(__float_as_uint(x), __float_as_uint(x), false, false);
  x = fmaxf(__uint_as_float(s[0]), __uint_as_float(s[1]));
  auto t = __builtin_amdgcn_permlane32_swap(__float_as_uint(x), __float_as_uint(x), false, false);
  return fmaxf(__uint_as_float(t[0]), __uint_as_float(t[1]));
}
__device__ __forceinline__ float xrow16_sum(float x) {
  auto s = __builtin_amdgcn_permlane16_swap(__float_as_uint(x), __float_as_uint(x), false, false);
  x = __uint_as_float(s[0]) + __uint_as_float(s[1]);
  auto t = __builtin_amdgcn_permlane32_swap(__float_as_uint(x), __float_as_uint(x), false, false);
  return __uint_as_float(t[0]) + __uint_as_float(t[1]);
}
__device__ __forceinline__ float wave_sum(float v) {
  for (int o = 32; o >= 1; o >>= 1) v += __shfl_xor(v, o);
  return v;
}

__device__ void ln512(float* xs, float* red) {
  const int tid = threadIdx.x;
  float a = xs[tid], b = xs[tid + 256];
  float s = wave_sum(a + b);
  if ((tid & 63) == 0) red[tid >> 6] = s;
  __syncthreads();
  const float mean = (red[0] + red[1] + red[2] + red[3]) * (1.f / 512.f);
  __syncthreads();
  float da = a - mean, db = b - mean;
  s = wave_sum(da * da + db * db);
  if ((tid & 63) == 0) red[tid >> 6] = s;
  __syncthreads();
  const float var = (red[0] + red[1] + red[2] + red[3]) * (1.f / 512.f);
  const float rs = 1.f / sqrtf(var + 1e-5f);
  xs[tid] = rbf(da * rs); xs[tid + 256] = rbf(db * rs);
  __syncthreads();
}

__global__ void __launch_bounds__(256) k_qproj(const bf16_t* x, const bf16_t* wq, bf16_t* q) {
  __shared__ float xs[512]; __shared__ float red[4]; __shared__ float part[4][16];
  const int tid = threadIdx.x;
  const int c0 = blockIdx.x * 16;
  const int half = tid & 1, r0 = tid >> 1;
  u32x4 w[4];
#pragma unroll
  for (int i = 0; i < 4; ++i) w[i] = *reinterpret_cast<const u32x4*>(wq + (size_t)(r0 + 128 * i) * 4096 + c0 + half * 8);
  xs[tid] = bf2f(x[tid]); xs[tid + 256] = bf2f(x[tid + 256]);
  __syncthreads();
  ln512(xs, red);
  float acc[8];
#pragma unroll
  for (int k = 0; k < 8; ++k) acc[k] = 0.f;
#pragma unroll
  for (int i = 0; i < 4; ++i) { const float xv = xs[r0 + 128 * i];
#pragma unroll
    for (int k = 0; k < 4; ++k) { acc[2 * k] += xv * __builtin_bit_cast(float, w[i][k] << 16); acc[2 * k + 1] += xv * __builtin_bit_cast(float, w[i][k] & 0xffff0000u); } }
#pragma unroll
  for (int k = 0; k < 8; ++k) { float v = acc[k]; for (int o = 32; o >= 2; o >>= 1) v += __shfl_xor(v, o); acc[k] = v; }
  if ((tid & 63) < 2) {
#pragma unroll
    for (int k = 0; k < 8; ++k) part[tid >> 6][half * 8 + k] = acc[k]; }
  __syncthreads();
  if (tid < 16) q[c0 + tid] = f2bf(part[0][tid] + part[1][tid] + part[2][tid] + part[3][tid]);
}

__global__ void __launch_bounds__(256) k_attn(const bf16_t* q, const bf16_t* kc, const bf16_t* vc, float* pml, float* po) {
  constexpr int MRG_REC = 4 + DH;
  __shared__ __attribute__((aligned(16))) float lds[WAVES * 64 + WAVES * 4 * MRG_REC];
  const int kvh = blockIdx.x & 7, s = blockIdx.x >> 3;
  const int tid = threadIdx.x, lane = tid & 63, wave = __builtin_amdgcn_readfirstlane(tid >> 6), r = lane >> 4, c = lane & 15;
  constexpr float C2 = 0.088388347648318440f * 1.4426950408889634f;
  constexpr int PPS = SK / 16 / NSPLIT, NWP = PPS / WAVES;
  u32x4 qpk[4];
#pragma unroll
  for (int g = 0; g < 4; ++g) qpk[g] = *reinterpret_cast<const u32x4*>(q + (kvh * 4 + g) * DH + 8 * c);
  const size_t base = ((size_t)kvh * SK + (size_t)s * PPS * 16 + wave * 16 + r) * DH + 8 * c;
  const bf16_t* kb = kc + base; const bf16_t* vb = vc + base;
  u32x4 Kn[4], Vn[4];
#define ISSUE(i_) do { _Pragma("unroll") for (int t_ = 0; t_ < 4; ++t_) { \
    Kn[t_] = __builtin_nontemporal_load(reinterpret_cast<const u32x4*>(kb + (size_t)(i_) * 64 * DH + t_ * 4 * DH)); \
    Vn[t_] = __builtin_nontemporal_load(reinterpret_cast<const u32x4*>(vb + (size_t)(i_) * 64 * DH + t_ * 4 * DH)); } } while (0)
  float m = -INFINITY, l = 0.f, acc[32];
#pragma unroll
  for (int k = 0; k < 32; ++k) acc[k] = 0.f;
  ISSUE(0);
  for (int i = 0; i < NWP; ++i) {
    u32x4 Kc[4], Vc[4];
#pragma unroll
    for (int t = 0; t < 4; ++t) { Kc[t] = Kn[t]; Vc[t] = Vn[t]; }
    if (i + 1 < NWP) ISSUE(i + 1);
    float x[16];
#pragma unroll
    for (int t = 0; t < 4; ++t)
#pragma unroll
      for (int g = 0; g < 4; ++g) { float d = 0.f;
#pragma unroll
        for (int k = 0; k < 4; ++k) d = dot2(Kc[t][k], qpk[g][k], d);
        x[t * 4 + g] = d; }
#define TR_STEP(HALF, CTRL, BIT) _Pragma("unroll") for (int h = 0; h < (HALF); ++h) { \
      const float lo_ = x[h], hi_ = x[h + (HALF)]; const float t1_ = lo_ + dpp<CTRL>(lo_), t2_ = hi_ + dpp<CTRL>(hi_); \
      x[h] = (c & (BIT)) ? t2_ : t1_; }
    TR_STEP(8, XOR8, 8) TR_STEP(4, XOR7, 4) TR_STEP(2, XOR2, 2) TR_STEP(1, XOR1, 1)
#undef TR_STEP
    const float sc = x[0] * C2;
    float pm = sc; pm = fmaxf(pm, dpp<ROR4>(pm)); pm = fmaxf(pm, dpp<XOR8>(pm)); pm = xrow16_max(pm);
    const float mn = fmaxf(m, pm);
    const float alpha = __builtin_amdgcn_exp2f(m - mn), p = __builtin_amdgcn_exp2f(sc - mn);
    l = fmaf(l, alpha, p); m = mn;
#pragma unroll
    for (int q4 = 0; q4 < 4; ++q4) {
      const float al = q4 == 0 ? dpp<0x00>(alpha) : q4 == 1 ? dpp<0x55>(alpha) : q4 == 2 ? dpp<0xAA>(alpha) : dpp<0xFF>(alpha);
#pragma unroll
      for (int k = 0; k < 8; ++k) acc[q4 * 8 + k] *= al; }
    float* prow = lds + (wave * 4 + r) * 16;
    prow[c] = p;
    asm volatile("s_waitcnt lgkmcnt(0)" ::: "memory");
    f32x4 pr[4];
#pragma unroll
    for (int u = 0; u < 4; ++u) pr[u] = *reinterpret_cast<const f32x4*>(prow + 4 * u);
#pragma unroll
    for (int t = 0; t < 4; ++t) {
      float vf[8];
#pragma unroll
      for (int k = 0; k < 4; ++k) { vf[2 * k] = __builtin_bit_cast(float, Vc[t][k] << 16); vf[2 * k + 1] = __builtin_bit_cast(float, Vc[t][k] & 0xffff0000u); }
#pragma unroll
      for (int g = 0; g < 4; ++g) { const float pg_ = pr[t][g];
#pragma unroll
        for (int k = 0; k < 8; ++k) acc[g * 8 + k] = fmaf(pg_, vf[k], acc[g * 8 + k]); } }
  }
#pragma unroll
  for (int k = 0; k < 32; ++k) acc[k] = xrow16_sum(acc[k]);
  { float t = l; t += dpp<ROR4>(t); t += dpp<XOR8>(t); l = xrow16_sum(t); }
  float* mrg = lds + WAVES * 64;
  if (r == 0) {
#pragma unroll
    for (int g = 0; g < 4; ++g) {
      float* rec = mrg + (wave * 4 + g) * MRG_REC;
#pragma unroll
      for (int k = 0; k < 8; k += 4) *reinterpret_cast<f32x4*>(rec + 4 + 8 * c + k) = f32x4{acc[g * 8 + k], acc[g * 8 + k + 1], acc[g * 8 + k + 2], acc[g * 8 + k + 3]};
      if (c == g) { rec[0] = m; rec[1] = l; } } }
  __syncthreads();
  {
    const int g = wave, d0 = 2 * lane;
    const int row = kvh * 4 + g;
    float M = -INFINITY;
#pragma unroll
    for (int w = 0; w < WAVES; ++w) M = fmaxf(M, mrg[(w * 4 + g) * MRG_REC]);
    float L = 0.f, o0 = 0.f, o1 = 0.f;
#pragma unroll
    for (int w = 0; w < WAVES; ++w) { const float* rec = mrg + (w * 4 + g) * MRG_REC; const float lw = rec[1];
      const float wt = __builtin_amdgcn_exp2f(rec[0] - M);
      L = fmaf(wt, lw, L); o0 = fmaf(wt, rec[4 + d0], o0); o1 = fmaf(wt, rec[4 + d0 + 1], o1); }
    const size_t slot = (size_t)row * NSPLIT + s;
    *reinterpret_cast<float2*>(po + slot * DH + d0) = make_float2(o0, o1);
    if (lane == 0) *reinterpret_cast<float2*>(pml + 2 * slot) = make_float2(M, L);
  }
}

__global__ void __launch_bounds__(256) k_comb_wo(const float* pml, const float* po, const bf16_t* wo, float* ypart) {
  __shared__ float wts[NSPLIT]; __shared__ float o[2][DH]; __shared__ float red[4][128];
  const int h = blockIdx.x >> 2, cq = blockIdx.x & 3, tid = threadIdx.x;
  u32x4 w[8];
#pragma unroll
  for (int i = 0; i < 8; ++i) w[i] = *reinterpret_cast<const u32x4*>(wo + (size_t)(h * 128 + (tid >> 4) + 16 * i) * 512 + cq * 128 + (tid & 15) * 8);
  if (tid < 64) {
    const float mm = pml[2 * (h * NSPLIT + tid)], ll = pml[2 * (h * NSPLIT + tid) + 1];
    float M = mm; for (int off = 32; off >= 1; off >>= 1) M = fmaxf(M, __shfl_xor(M, off));
    const float e = __builtin_amdgcn_exp2f(mm - M);
    const float L = wave_sum(e * ll);
    wts[tid] = e / L;
  }
  __syncthreads();
  { const int d = tid & 127, hf = tid >> 7; float a = 0.f;
    for (int s = hf * 32; s < hf * 32 + 32; ++s) a += wts[s] * po[((size_t)h * NSPLIT + s) * DH + d];
    o[hf][d] = a; }
  __syncthreads();
  float acc[8];
#pragma unroll
  for (int k = 0; k < 8; ++k) acc[k] = 0.f;
#pragma unroll
  for (int i = 0; i < 8; ++i) { const int r = (tid >> 4) + 16 * i; const float ov = rbf(o[0][r] + o[1][r]);
#pragma unroll
    for (int k = 0; k < 4; ++k) { acc[2 * k] += ov * __builtin_bit_cast(float, w[i][k] << 16); acc[2 * k + 1] += ov * __builtin_bit_cast(float, w[i][k] & 0xffff0000u); } }
#pragma unroll
  for (int k = 0; k < 8; ++k) { float v = acc[k]; v += __shfl_xor(v, 16); v += __shfl_xor(v, 32); acc[k] = v; }
  if ((tid & 63) < 16) {
#pragma unroll
    for (int k = 0; k < 8; ++k) red[tid >> 6][(tid & 15) * 8 + k] = acc[k]; }
  __syncthreads();
  if (tid < 128) ypart[h * 512 + cq * 128 + tid] = red[0][tid] + red[1][tid] + red[2][tid] + red[3][tid];
}

__global__ void __launch_bounds__(256) k_gateup(const bf16_t* x, const float* ypart, const bf16_t* wg, const bf16_t* wu, float* act, float* hout) {
  __shared__ float xs[512]; __shared__ float red[4]; __shared__ float part[4][32];
  const int tid = threadIdx.x, c0 = blockIdx.x * 16;
  const int half = tid & 1, r0 = tid >> 1;
  u32x4 w[4], u[4];
#pragma unroll
  for (int i = 0; i < 4; ++i) { w[i] = *reinterpret_cast<const u32x4*>(wg + (size_t)(r0 + 128 * i) * F + c0 + half * 8);
    u[i] = *reinterpret_cast<const u32x4*>(wu + (size_t)(r0 + 128 * i) * F + c0 + half * 8); }
  float a = 0.f, b = 0.f;
#pragma unroll 8
  for (int hh = 0; hh < 32; ++hh) { a += ypart[hh * 512 + tid]; b += ypart[hh * 512 + tid + 256]; }
  a = rbf(bf2f(x[tid]) + rbf(a)); b = rbf(bf2f(x[tid + 256]) + rbf(b));
  if (blockIdx.x == 0) { hout[tid] = a; hout[tid + 256] = b; }
  xs[tid] = a; xs[tid + 256] = b;
  __syncthreads();
  ln512(xs, red);
  float acc[16];
#pragma unroll
  for (int k = 0; k < 16; ++k) acc[k] = 0.f;
#pragma unroll
  for (int i = 0; i < 4; ++i) { const float xv = xs[r0 + 128 * i];
#pragma unroll
    for (int k = 0; k < 4; ++k) { acc[2 * k] += xv * __builtin_bit_cast(float, w[i][k] << 16); acc[2 * k + 1] += xv * __builtin_bit_cast(float, w[i][k] & 0xffff0000u);
      acc[8 + 2 * k] += xv * __builtin_bit_cast(float, u[i][k] << 16); acc[8 + 2 * k + 1] += xv * __builtin_bit_cast(float, u[i][k] & 0xffff0000u); } }
#pragma unroll
  for (int k = 0; k < 16; ++k) { float v = acc[k]; for (int o = 32; o >= 2; o >>= 1) v += __shfl_xor(v, o); acc[k] = v; }
  if ((tid & 63) < 2) {
#pragma unroll
    for (int k = 0; k < 8; ++k) { part[tid >> 6][half * 8 + k] = acc[k]; part[tid >> 6][16 + half * 8 + k] = acc[8 + k]; } }
  __syncthreads();
  if (tid < 16) { const float g = rbf(part[0][tid] + part[1][tid] + part[2][tid] + part[3][tid]);
    const float uu = rbf(part[0][16 + tid] + part[1][16 + tid] + part[2][16 + tid] + part[3][16 + tid]);
    act[c0 + tid] = rbf((g > 0.f ? g : 0.f) * uu); }
}

__global__ void __launch_bounds__(256) k_down(const float* act, const float* hin, const bf16_t* wd, bf16_t* out) {
  __shared__ float part[4][8];
  const int tid = threadIdx.x, c0 = blockIdx.x * 8;
  u32x4 w[7];
#pragma unroll
  for (int i = 0; i < 7; ++i) w[i] = *reinterpret_cast<const u32x4*>(wd + (size_t)(tid + 256 * i) * 512 + c0);
  float acc[8];
#pragma unroll
  for (int k = 0; k < 8; ++k) acc[k] = 0.f;
#pragma unroll
  for (int i = 0; i < 7; ++i) { const float xv = act[tid + 256 * i];
#pragma unroll
    for (int k = 0; k < 4; ++k) { acc[2 * k] += xv * __builtin_bit_cast(float, w[i][k] << 16); acc[2 * k + 1] += xv * __builtin_bit_cast(float, w[i][k] & 0xffff0000u); } }
#pragma unroll
  for (int k = 0; k < 8; ++k) acc[k] = wave_sum(acc[k]);
  if ((tid & 63) == 0) {
#pragma unroll
    for (int k = 0; k < 8; ++k) part[tid >> 6][k] = acc[k]; }
  __syncthreads();
  if (tid < 8) out[c0 + tid] = f2bf(hin[c0 + tid] + rbf(part[0][tid] + part[1][tid] + part[2][tid] + part[3][tid]));
}

extern "C" void kernel_launch(void* const* d_in, const int* in_sizes, int n_in, void* d_out, int out_size, void* d_ws, size_t ws_size, hipStream_t stream) {
  const bf16_t* x = (const bf16_t*)d_in[0];
  const bf16_t* kc = (const bf16_t*)d_in[1];
  const bf16_t* vc = (const bf16_t*)d_in[2];
  const bf16_t* wq = (const bf16_t*)d_in[3];
  const bf16_t* wo = (const bf16_t*)d_in[4];
  const bf16_t* wg = (const bf16_t*)d_in[6];
  const bf16_t* wu = (const bf16_t*)d_in[7];
  const bf16_t* wd = (const bf16_t*)d_in[8];
  char* ws = (char*)d_ws;
  bf16_t* q = (bf16_t*)ws;
  float* pml = (float*)(ws + 16384);
  float* po = (float*)(ws + 65536);
  float* ypart = (float*)(ws + 65536 + (1 << 20));
  float* act = (float*)(ws + 65536 + (1 << 20) + 65536);
  float* hbuf = act + 2048;
  k_qproj<<<256, 256, 0, stream>>>(x, wq, q);
  k_attn<<<8 * NSPLIT, 256, 0, stream>>>(q, kc, vc, pml, po);
  k_comb_wo<<<128, 256, 0, stream>>>(pml, po, wo, ypart);
  k_gateup<<<112, 256, 0, stream>>>(x, ypart, wg, wu, act, hbuf);
  k_down<<<64, 256, 0, stream>>>(act, hbuf, wd, (bf16_t*)d_out);
}
```

```cpp
#include <hip/hip_runtime.h>
#include <cstdint>

typedef unsigned short bf16_t;
typedef __attribute__((ext_vector_type(2))) __bf16 bf16x2;
typedef __attribute__((ext_vector_type(4))) __bf16 bf16x4;
typedef __attribute__((ext_vector_type(8))) __bf16 bf16x8;
typedef __attribute__((ext_vector_type(2))) unsigned u32x2;
typedef __attribute__((ext_vector_type(4))) unsigned u32x4;
typedef __attribute__((ext_vector_type(2))) float f32x2;
typedef __attribute__((ext_vector_type(4))) float f32x4;
typedef unsigned long long u64;
#define GAS __attribute__((address_space(1)))
#define LAS __attribute__((address_space(3)))
#define RLX_AGENT __ATOMIC_RELAXED, __HIP_MEMORY_SCOPE_AGENT

#define WIN 24

constexpr int SK = 65536, F = 1792;
constexpr int NWG = 256, NTHR = 320, NTILE = 64;
constexpr unsigned SENT = 0xFFFFFFFFu;
constexpr int WS_E = 0, WS_QG = 4096, WS_P1 = 65536, P1_REC = 2176, WS_Y = WS_P1 + NWG * P1_REC, WS_ACT = WS_Y + 65536, WS_H = WS_ACT + 4096, WS_END = WS_H + 4096;
constexpr int WS_TS = 1 << 20;
constexpr int L_CTRL = 131072, L_Q = L_CTRL + 256, L_QP = L_Q + 1024, L_MRG = L_QP + 256, MRG_REC = 132 * 4, L_END = L_MRG + 16 * MRG_REC;
constexpr int L_SCR = 65536;
constexpr int C_LANDED = 0, C_CONS = 4, C_QSYNC = 32, C_QRDY = 36;
constexpr float LOG2E = 1.4426950408889634f;
constexpr int SPIN_G = 400000, SPIN_L = 8000000;

__device__ __forceinline__ float bflo(unsigned u) { return __builtin_bit_cast(float, u << 16); }
__device__ __forceinline__ float bfhi(unsigned u) { return __builtin_bit_cast(float, u & 0xffff0000u); }
__device__ __forceinline__ unsigned pk2(float lo, float hi) { bf16x2 v = {(__bf16)lo, (__bf16)hi}; return __builtin_bit_cast(unsigned, v); }
__device__ __forceinline__ float rbf(float f) { return (float)(__bf16)f; }
__device__ __forceinline__ unsigned nosent(unsigned u) { return u == SENT ? 0x7FFF7FFFu : u; }
__device__ __forceinline__ unsigned fns(float f) { return nosent(__builtin_bit_cast(unsigned, f)); }
template <int CTRL> __device__ __forceinline__ float dpp(float x) { return __builtin_bit_cast(float, __builtin_amdgcn_mov_dpp(__builtin_bit_cast(int, x), CTRL, 0xf, 0xf, true)); }
constexpr int XOR1 = 0xB1, XOR2 = 0x4E, HMIR = 0x141, ROR8 = 0x128;
__device__ __forceinline__ float x16_max(float x) { auto s = __builtin_amdgcn_permlane16_swap(__float_as_uint(x), __float_as_uint(x), false, false); return fmaxf(__uint_as_float(s[0]), __uint_as_float(s[1])); }
__device__ __forceinline__ float x32_max(float x) { auto s = __builtin_amdgcn_permlane32_swap(__float_as_uint(x), __float_as_uint(x), false, false); return fmaxf(__uint_as_float(s[0]), __uint_as_float(s[1])); }
__device__ __forceinline__ float x16_sum(float x) { auto s = __builtin_amdgcn_permlane16_swap(__float_as_uint(x), __float_as_uint(x), false, false); return __uint_as_float(s[0]) + __uint_as_float(s[1]); }
__device__ __forceinline__ float x32_sum(float x) { auto s = __builtin_amdgcn_permlane32_swap(__float_as_uint(x), __float_as_uint(x), false, false); return __uint_as_float(s[0]) + __uint_as_float(s[1]); }
__device__ __forceinline__ float wave_sum(float x) { x += dpp<XOR1>(x); x += dpp<XOR2>(x); x += dpp<HMIR>(x); x += dpp<ROR8>(x); return x32_sum(x16_sum(x)); }
template <int N> __device__ __forceinline__ void waitvm() { asm volatile("s_waitcnt vmcnt(%0)" :: "i"(N) : "memory"); }
__device__ __forceinline__ void lds_st(unsigned addr, unsigned v) { asm volatile("ds_write_b32 %0, %1" :: "v"(addr), "v"(v) : "memory"); }
__device__ __forceinline__ unsigned lds_ld(unsigned addr) { unsigned v; asm volatile("ds_read_b32 %0, %1\n\ts_waitcnt lgkmcnt(0)" : "=&v"(v) : "v"(addr) : "memory"); return __builtin_amdgcn_readfirstlane(v); }
__device__ __forceinline__ void lds_wait_ge(unsigned addr, unsigned want) {
#pragma unroll 1
  for (int sp = 0; sp < SPIN_L; ++sp) { if (lds_ld(addr) >= want) break; __builtin_amdgcn_s_sleep(1); } }
__device__ __forceinline__ bool ok4(u32x4 v) { return (v[0] != SENT) & (v[1] != SENT) & (v[2] != SENT) & (v[3] != SENT); }
__device__ __forceinline__ bf16x8 asb8(u32x4 v) { return __builtin_bit_cast(bf16x8, v); }
__device__ __forceinline__ f32x4 mfma(bf16x8 a, bf16x8 b, f32x4 c) { return __builtin_amdgcn_mfma_f32_16x16x32_bf16(a, b, c, 0, 0, 0); }
__device__ __forceinline__ bf16x8 tr2(LAS char* p0, LAS char* p1) {
  bf16x4 a = __builtin_amdgcn_ds_read_tr16_b64_v4bf16((LAS bf16x4*)p0), b = __builtin_amdgcn_ds_read_tr16_b64_v4bf16((LAS bf16x4*)p1);
  return __builtin_shufflevector(a, b, 0, 1, 2, 3, 4, 5, 6, 7);
}
#define GLDS(src, dst, off) __builtin_amdgcn_global_load_lds((const GAS unsigned*)(src), (LAS unsigned*)(dst), 16, (off), 2)
#define ST(i) do {} while (0)

__device__ __forceinline__ void ln8(const float (&v)[8], float (&o)[8]) {
  float s = ((v[0] + v[1]) + (v[2] + v[3])) + ((v[4] + v[5]) + (v[6] + v[7]));
  const float mean = wave_sum(s) * (1.f / 512.f);
  float q = 0.f;
#pragma unroll
  for (int i = 0; i < 8; ++i) { const float d = v[i] - mean; q = fmaf(d, d, q); }
  const float var = wave_sum(q) * (1.f / 512.f);
  const float mb = rbf(mean), rsd = __builtin_amdgcn_rcpf(rbf(__builtin_amdgcn_sqrtf(rbf(rbf(var) + 1e-5f))));
#pragma unroll
  for (int i = 0; i < 8; ++i) o[i] = rbf(rbf(v[i] - mb) * rsd);
}

__global__ void __launch_bounds__(NTHR, 1) fused_decode(const bf16_t* __restrict__ x, const bf16_t* __restrict__ kc, const bf16_t* __restrict__ vc, const bf16_t* __restrict__ wq,
                                                        const bf16_t* __restrict__ wo, const bf16_t* __restrict__ wg, const bf16_t* __restrict__ wu, const bf16_t* __restrict__ wd,
                                                        bf16_t* __restrict__ out, char* ws) {
  __shared__ __attribute__((aligned(1024))) char lds_raw[L_END];
  LAS char* L = (LAS char*)lds_raw;
  const unsigned lbase = (unsigned)(size_t)L;
  const int tid = threadIdx.x, lane = tid & 63, wave = __builtin_amdgcn_readfirstlane(tid >> 6), bid = blockIdx.x;
  const int g = bid & 7, s = bid >> 3, rec = g * 32 + s;
  ST(0);

  if (wave == 4) {
    const int r4 = lane >> 4;
    const unsigned off_e = r4 * 256 + (((lane & 15) ^ (r4 << 1)) << 4), off_o = off_e ^ 128;
    const char* kb = (const char*)kc + ((size_t)g * SK + (size_t)s * 2048) * 256;
    const char* vb = (const char*)vc + ((size_t)g * SK + (size_t)s * 2048) * 256;
    if (lane < 16) lds_st(lbase + L_CTRL + lane * 4, 0u);
    int pub = 0;
#pragma unroll 1
    for (int t = 0; t < NTILE; ++t) {
      if (t == 1) { asm volatile("s_waitcnt lgkmcnt(0)" ::: "memory"); __builtin_amdgcn_s_barrier(); }
      if (t >= 8) {
        const unsigned need = (unsigned)(((t - 8) >> 2) + 1), ca = lbase + L_CTRL + C_CONS + (t & 3) * 4;
        if (lds_ld(ca) < need) {
          waitvm<8>(); lds_st(lbase + L_CTRL + C_LANDED, 2 * t - 1); waitvm<0>(); pub = 2 * t; lds_st(lbase + L_CTRL + C_LANDED, pub);
          lds_wait_ge(ca, need);
        }
      }
      LAS char* dst = L + (t & 7) * 16384;
      const char* ks = kb + (size_t)t * 8192; const char* vs = vb + (size_t)t * 8192;
#pragma unroll
      for (int u = 0; u < 4; ++u) {
        waitvm<WIN - 4>();
        const int h = (t * 16 + u * 4 - (WIN - 4)) >> 3;
        if (h > pub) { pub = h; lds_st(lbase + L_CTRL + C_LANDED, pub); }
        const char* p = (u < 2 ? ks : vs) + (u & 1) * 4096;
        GLDS(p + off_e, dst + u * 4096, 0); GLDS(p + off_o, dst + u * 4096, 1024); GLDS(p + off_e, dst + u * 4096, 2048); GLDS(p + off_o, dst + u * 4096, 3072);
      }
    }
    ST(1);
    waitvm<16>(); lds_st(lbase + L_CTRL + C_LANDED, 2 * NTILE - 2);
    waitvm<8>(); lds_st(lbase + L_CTRL + C_LANDED, 2 * NTILE - 1);
    waitvm<0>(); lds_st(lbase + L_CTRL + C_LANDED, 2 * NTILE);
    asm volatile("s_waitcnt lgkmcnt(0)" ::: "memory");
    ST(2);
    return;
  }

  const __amdgpu_buffer_rsrc_t rs = __builtin_amdgcn_make_buffer_rsrc(ws, 0, WS_END, 0x00020000);
  const int n16 = lane & 15, gq = lane >> 4;
  const bool roleA = s < 8, roleB = s >= 8 && s < 15, roleC = s >= 16 && s < 20;
  const int bi = g * 7 + (s - 8), ci = g * 4 + (s - 16), hq = 4 * g + (s >> 1), ch = s & 1;
  u64 tag; u64 epoch;
  u32x4 xv;
  {
    u32x4 w[4];
    const int half = tid & 1, r0 = tid >> 1;
#pragma unroll
    for (int i = 0; i < 4; ++i) w[i] = __builtin_nontemporal_load((const u32x4*)(wq + (size_t)(r0 + 128 * i) * 4096 + g * 512 + s * 16 + half * 8));
    xv = *(const u32x4*)(x + lane * 8);
    u32x2 ev = __builtin_amdgcn_raw_buffer_load_b64(rs, WS_E, 0, 16);
    const u32x4 sv = {SENT, SENT, SENT, SENT};
    if (tid < 136) __builtin_amdgcn_raw_buffer_store_b128(sv, rs, WS_P1 + rec * P1_REC + tid * 16, 0, 16);
    if (roleA && wave == 3) __builtin_amdgcn_raw_buffer_store_b128(sv, rs, WS_Y + hq * 2048 + ch * 1024 + lane * 16, 0, 16);
    if (roleB && wave == 3 && lane < 8) __builtin_amdgcn_raw_buffer_store_b128(sv, rs, (lane < 4 ? WS_ACT : WS_H) + bi * 64 + (lane & 3) * 16, 0, 16);
    __builtin_amdgcn_s_barrier();
    asm volatile("" : "+v"(xv), "+v"(ev), "+v"(w[0]), "+v"(w[1]), "+v"(w[2]), "+v"(w[3]));
    epoch = ((u64)(unsigned)__builtin_amdgcn_readfirstlane(ev[1]) << 32) | (unsigned)__builtin_amdgcn_readfirstlane(ev[0]);
    float xf[8], xn[8];
#pragma unroll
    for (int k = 0; k < 4; ++k) { xf[2 * k] = bflo(xv[k]); xf[2 * k + 1] = bfhi(xv[k]); }
    ln8(xf, xn);
    LAS float* xs = (LAS float*)(L + L_MRG + wave * 2048);
    *(LAS f32x4*)(xs + lane * 8) = f32x4{xn[0], xn[1], xn[2], xn[3]};
    *(LAS f32x4*)(xs + lane * 8 + 4) = f32x4{xn[4], xn[5], xn[6], xn[7]};
    float acc[8];
#pragma unroll
    for (int k = 0; k < 8; ++k) acc[k] = 0.f;
#pragma unroll
    for (int i = 0; i < 4; ++i) { const float xr = xs[r0 + 128 * i];
#pragma unroll
      for (int k = 0; k < 4; ++k) { acc[2 * k] = fmaf(xr, bflo(w[i][k]), acc[2 * k]); acc[2 * k + 1] = fmaf(xr, bfhi(w[i][k]), acc[2 * k + 1]); } }
#pragma unroll
    for (int k = 0; k < 8; ++k) { float v = acc[k]; v += dpp<XOR2>(v); v += dpp<0x124>(v); v += dpp<ROR8>(v); acc[k] = x32_sum(x16_sum(v)); }
    u64 z = epoch + 0x9E3779B97F4A7C15ull; z = (z ^ (z >> 30)) * 0xBF58476D1CE4E5B9ull; z = (z ^ (z >> 27)) * 0x94D049BB133111EBull; z ^= z >> 31;
    tag = z & 0xFFFFFFFFFFFF0000ull;
    waitvm<0>();
    LAS float* qp = (LAS float*)(L + L_QP);
    if (lane < 2) { *(LAS f32x4*)(qp + wave * 16 + lane * 8) = f32x4{acc[0], acc[1], acc[2], acc[3]}; *(LAS f32x4*)(qp + wave * 16 + lane * 8 + 4) = f32x4{acc[4], acc[5], acc[6], acc[7]}; }
    asm volatile("s_waitcnt lgkmcnt(0)" ::: "memory");
    if (lane == 0) __hip_atomic_fetch_add((LAS unsigned*)(L + L_CTRL + C_QSYNC), 1u, __ATOMIC_RELAXED, __HIP_MEMORY_SCOPE_WORKGROUP);
    ST(1);
    if (wave == 0) {
      lds_wait_ge(lbase + L_CTRL + C_QSYNC, 4u);
      if (lane < 16) {
        const float qv = (qp[lane] + qp[16 + lane]) + (qp[32 + lane] + qp[48 + lane]);
        __hip_atomic_store((GAS u64*)(ws + WS_QG + rec * 128 + lane * 8), tag | (u64)(pk2(qv, 0.f) & 0xffffu), RLX_AGENT);
      }
      ST(2);
      const unsigned thi = (unsigned)(tag >> 32), tlo = (unsigned)tag;
      u32x4 v[4];
#pragma unroll 1
      for (int sp = 0; sp < SPIN_G; ++sp) {
        asm volatile("" ::: "memory");
        bool ok = true;
#pragma unroll
        for (int i = 0; i < 4; ++i) v[i] = __builtin_amdgcn_raw_buffer_load_b128(rs, WS_QG + g * 4096 + i * 1024 + lane * 16, 0, 16);
#pragma unroll
        for (int i = 0; i < 4; ++i) ok &= (v[i][1] == thi) & (v[i][3] == thi) & ((v[i][0] & 0xffff0000u) == tlo) & ((v[i][2] & 0xffff0000u) == tlo);
        if (__all(ok)) break;
        __builtin_amdgcn_s_sleep(1);
      }
#pragma unroll
      for (int i = 0; i < 4; ++i) *(LAS unsigned*)(L + L_Q + (i * 64 + lane) * 4) = (v[i][0] & 0xffffu) | (v[i][2] << 16);
      asm volatile("s_waitcnt lgkmcnt(0)" ::: "memory");
      lds_st(lbase + L_CTRL + C_QRDY, 1u);
    } else {
      lds_wait_ge(lbase + L_CTRL + C_QRDY, 1u);
    }
  }
  ST(3);
  bf16x8 qf[4];
#pragma unroll
  for (int j = 0; j < 4; ++j) { u32x4 v = *(LAS u32x4*)(L + L_Q + (n16 & 3) * 256 + j * 64 + gq * 16); if (n16 >= 4) v = u32x4{0u, 0u, 0u, 0u}; qf[j] = asb8(v); }

  unsigned koff[4], voff[8];
#pragma unroll
  for (int j = 0; j < 4; ++j) koff[j] = n16 * 256 + (((gq ^ ((lane & 7) << 1)) ^ (4 * j)) << 4);
  { const int q4 = n16 >> 2, p = lane & 3, r7 = 4 * (gq & 1) + q4;
#pragma unroll
    for (int c = 0; c < 8; ++c) voff[c] = 8192 + (4 * gq + q4) * 256 + ((c ^ r7) << 5) + p * 8; }
  float m = -3.0e38f, l = 0.f;
  f32x4 acc[8];
#pragma unroll
  for (int c = 0; c < 8; ++c) acc[c] = f32x4{0.f, 0.f, 0.f, 0.f};
#pragma unroll 1
  for (int it = 0; it < NTILE / 4; ++it) {
    const int t = it * 4 + wave;
    LAS char* sl = L + (t & 7) * 16384;
    lds_wait_ge(lbase + L_CTRL + C_LANDED, 2 * t + 1);
    f32x4 s0 = {0.f, 0.f, 0.f, 0.f}, s1 = {0.f, 0.f, 0.f, 0.f};
#pragma unroll
    for (int j = 0; j < 4; ++j) {
      s0 = mfma(asb8(*(LAS u32x4*)(sl + koff[j])), qf[j], s0);
      s1 = mfma(asb8(*(LAS u32x4*)(sl + koff[j] + 4096)), qf[j], s1);
    }
    float sc[8];
#pragma unroll
    for (int i = 0; i < 4; ++i) { sc[i] = rbf(rbf(s0[i]) * 0.08838834764831845f); sc[4 + i] = rbf(rbf(s1[i]) * 0.08838834764831845f); }
    float tm = fmaxf(fmaxf(fmaxf(sc[0], sc[1]), fmaxf(sc[2], sc[3])), fmaxf(fmaxf(sc[4], sc[5]), fmaxf(sc[6], sc[7])));
    tm = x32_max(x16_max(tm));
    const float mn = fmaxf(m, tm);
    const float alpha = __builtin_amdgcn_exp2f((m - mn) * LOG2E);
    float p[8], ps = 0.f;
#pragma unroll
    for (int i = 0; i < 8; ++i) { p[i] = __builtin_amdgcn_exp2f((sc[i] - mn) * LOG2E); ps += p[i]; }
    l = fmaf(l, alpha, ps); m = mn;
    const bf16x8 pf = asb8(u32x4{pk2(p[0], p[1]), pk2(p[2], p[3]), pk2(p[4], p[5]), pk2(p[6], p[7])});
    lds_wait_ge(lbase + L_CTRL + C_LANDED, 2 * t + 2);
    bf16x8 vf[8];
#pragma unroll
    for (int c = 0; c < 8; ++c) vf[c] = tr2(sl + voff[c], sl + voff[c] + 4096);
    asm volatile("s_waitcnt lgkmcnt(0)" : "+v"(vf[0]), "+v"(vf[1]), "+v"(vf[2]), "+v"(vf[3]), "+v"(vf[4]), "+v"(vf[5]), "+v"(vf[6]), "+v"(vf[7]) :: "memory");
    lds_st(lbase + L_CTRL + C_CONS + wave * 4, it + 1);
#pragma unroll
    for (int c = 0; c < 8; ++c) acc[c] = mfma(vf[c], pf, acc[c] * alpha);
  }
  ST(4);
  l = x32_sum(x16_sum(l));
  if (n16 < 4) {
    LAS float* r = (LAS float*)(L + L_MRG + (wave * 4 + n16) * MRG_REC);
#pragma unroll
    for (int c = 0; c < 8; ++c) *(LAS f32x4*)(r + 4 + 16 * c + 4 * gq) = acc[c];
    if (gq == 0) *(LAS f32x2*)r = f32x2{m, l};
  }
  __syncthreads();
  {
    f32x2 ml[4], o[4];
#pragma unroll
    for (int w = 0; w < 4; ++w) { LAS float* r = (LAS float*)(L + L_MRG + (w * 4 + wave) * MRG_REC); ml[w] = *(LAS f32x2*)r; o[w] = *(LAS f32x2*)(r + 4 + 2 * lane); }
    const float M = fmaxf(fmaxf(ml[0][0], ml[1][0]), fmaxf(ml[2][0], ml[3][0]));
    float Ls = 0.f, o0 = 0.f, o1 = 0.f;
#pragma unroll
    for (int w = 0; w < 4; ++w) { const float e = __builtin_amdgcn_exp2f((ml[w][0] - M) * LOG2E); Ls = fmaf(e, ml[w][1], Ls); o0 = fmaf(e, o[w][0], o0); o1 = fmaf(e, o[w][1], o1); }
    const float inv = __builtin_amdgcn_rcpf(Ls);
    __builtin_amdgcn_raw_buffer_store_b64(u32x2{fns(o0 * inv), fns(o1 * inv)}, rs, WS_P1 + rec * P1_REC + wave * 512 + lane * 8, 0, 16);
    if (lane == 0) __builtin_amdgcn_raw_buffer_store_b32(fns(fmaf(M, LOG2E, __builtin_amdgcn_logf(Ls))), rs, WS_P1 + rec * P1_REC + 2048 + wave * 4, 0, 16);
  }
  ST(5);
  if (!(roleA || roleB || roleC)) return;
  const int q4 = n16 >> 2, p4 = lane & 3;
  bf16x8 wf[16];

  if (roleA) {
    LAS char* sl = L + wave * 16384;
    const char* src = (const char*)wo + (size_t)(hq * 128 + (lane >> 3)) * 1024 + ch * 512 + wave * 128 + (lane & 7) * 16;
#pragma unroll
    for (int i = 0; i < 16; ++i) GLDS(src + i * 8192, sl + i * 1024, 0);
    waitvm<0>();
#pragma unroll
    for (int ct = 0; ct < 4; ++ct)
#pragma unroll
      for (int j = 0; j < 4; ++j) { LAS char* a = sl + (32 * j + 8 * gq + q4) * 128 + ct * 32 + p4 * 8; wf[ct * 4 + j] = tr2(a, a + 512); }
    ST(6);
    const int pb = WS_P1 + (g * 32 + 8 * wave) * P1_REC;
    u32x2 ov[8]; unsigned lse;
#pragma unroll 1
    for (int sp = 0; sp < SPIN_G; ++sp) {
      asm volatile("" ::: "memory");
#pragma unroll
      for (int i = 0; i < 8; ++i) ov[i] = __builtin_amdgcn_raw_buffer_load_b64(rs, pb + i * P1_REC + (hq & 3) * 512 + lane * 8, 0, 16);
      lse = __builtin_amdgcn_raw_buffer_load_b32(rs, pb + (lane & 7) * P1_REC + 2048 + (hq & 3) * 4, 0, 16);
      bool ok = lse != SENT;
#pragma unroll
      for (int i = 0; i < 8; ++i) ok &= (ov[i][0] != SENT) & (ov[i][1] != SENT);
      if (__all(ok)) break;
    }
    ST(7);
    const float ls = __uint_as_float(lse);
    float M = fmaxf(ls, dpp<XOR1>(ls)); M = fmaxf(M, dpp<XOR2>(M)); M = fmaxf(M, dpp<HMIR>(M));
    const float e = __builtin_amdgcn_exp2f(ls - M);
    float Lw = e + dpp<XOR1>(e); Lw += dpp<XOR2>(Lw); Lw += dpp<HMIR>(Lw);
    float o0 = 0.f, o1 = 0.f;
#pragma unroll
    for (int i = 0; i < 8; ++i) { const float ei = __builtin_bit_cast(float, __builtin_amdgcn_readlane(__builtin_bit_cast(int, e), i)); o0 = fmaf(ei, __uint_as_float(ov[i][0]), o0); o1 = fmaf(ei, __uint_as_float(ov[i][1]), o1); }
    LAS float* ao = (LAS float*)(L + L_SCR);
    *(LAS f32x2*)(ao + wave * 132 + 4 + 2 * lane) = f32x2{o0, o1};
    if (lane == 0) *(LAS f32x2*)(ao + wave * 132) = f32x2{M, Lw};
    __syncthreads();
    f32x2 ml[4], o[4];
#pragma unroll
    for (int w = 0; w < 4; ++w) { ml[w] = *(LAS f32x2*)(ao + w * 132); o[w] = *(LAS f32x2*)(ao + w * 132 + 4 + 2 * lane); }
    const float MM = fmaxf(fmaxf(ml[0][0], ml[1][0]), fmaxf(ml[2][0], ml[3][0]));
    float Ls = 0.f; o0 = 0.f; o1 = 0.f;
#pragma unroll
    for (int w = 0; w < 4; ++w) { const float f = __builtin_amdgcn_exp2f(ml[w][0] - MM); Ls = fmaf(f, ml[w][1], Ls); o0 = fmaf(f, o[w][0], o0); o1 = fmaf(f, o[w][1], o1); }
    const float inv = __builtin_amdgcn_rcpf(Ls);
    LAS char* vec = L + L_SCR + 4096 + wave * 256;
    *(LAS unsigned*)(vec + lane * 4) = pk2(o0 * inv, o1 * inv);
    f32x4 y[4];
#pragma unroll
    for (int ct = 0; ct < 4; ++ct) y[ct] = f32x4{0.f, 0.f, 0.f, 0.f};
#pragma unroll
    for (int j = 0; j < 4; ++j) { const bf16x8 b = asb8(*(LAS u32x4*)(vec + j * 64 + gq * 16));
#pragma unroll
      for (int ct = 0; ct < 4; ++ct) y[ct] = mfma(wf[ct * 4 + j], b, y[ct]); }
    const f32x4 yy = (n16 & 2) ? ((n16 & 1) ? y[3] : y[2]) : ((n16 & 1) ? y[1] : y[0]);
    if (n16 < 4) __builtin_amdgcn_raw_buffer_store_b128(u32x4{fns(yy[0]), fns(yy[1]), fns(yy[2]), fns(yy[3])}, rs, WS_Y + hq * 2048 + (ch * 256 + wave * 64 + n16 * 16 + gq * 4) * 4, 0, 16);
    ST(8);
    return;
  }

  auto check_tags = [&]() {
#pragma unroll 1
    for (int sp = 0; sp < SPIN_G; ++sp) {
      asm volatile("" ::: "memory");
      const u32x2 v = __builtin_amdgcn_raw_buffer_load_b64(rs, WS_QG + tid * 128, 0, 16);
      if (__all((v[1] == (unsigned)(tag >> 32)) & ((v[0] & 0xffff0000u) == (unsigned)tag))) break;
      __builtin_amdgcn_s_sleep(1);
    }
  };

  if (roleB) {
    const char* src = (const char*)((wave >> 1) ? wu : wg) + (size_t)((wave & 1) * 256 + (lane >> 2)) * (F * 2) + bi * 64 + (lane & 3) * 16;
#pragma unroll
    for (int i = 0; i < 16; ++i) GLDS(src + i * 16 * (F * 2), L + wave * 16384 + i * 1024, 0);
    check_tags();
    waitvm<0>();
    __syncthreads();
#pragma unroll
    for (int j = 0; j < 16; ++j) { LAS char* a = L + (p4 >> 1) * 32768 + (32 * j + 8 * gq + q4) * 64 + wave * 16 + (p4 & 1) * 8; wf[j] = tr2(a, a + 256); }
    ST(6);
    u32x4 yv[8][2];
    unsigned pend = 0xffu;
#pragma unroll 1
    for (int sp = 0; sp < SPIN_G && pend; ++sp) {
      asm volatile("" ::: "memory");
#pragma unroll
      for (int k = 0; k < 8; ++k) if (pend & (1u << k)) {
        yv[k][0] = __builtin_amdgcn_raw_buffer_load_b128(rs, WS_Y + (4 * k + wave) * 2048 + lane * 16, 0, 16);
        yv[k][1] = __builtin_amdgcn_raw_buffer_load_b128(rs, WS_Y + (4 * k + wave) * 2048 + 1024 + lane * 16, 0, 16); }
#pragma unroll
      for (int k = 0; k < 8; ++k) if ((pend & (1u << k)) && __all(ok4(yv[k][0]) & ok4(yv[k][1]))) pend &= ~(1u << k);
    }
    ST(7);
    f32x4 a0 = __builtin_bit_cast(f32x4, yv[0][0]), a1 = __builtin_bit_cast(f32x4, yv[0][1]);
#pragma unroll
    for (int k = 1; k < 8; ++k) { a0 += __builtin_bit_cast(f32x4, yv[k][0]); a1 += __builtin_bit_cast(f32x4, yv[k][1]); }
    LAS float* red = (LAS float*)(L + L_SCR);
    *(LAS f32x4*)(red + wave * 512 + lane * 4) = a0; *(LAS f32x4*)(red + wave * 512 + 256 + lane * 4) = a1;
    __syncthreads();
    f32x4 b0 = *(LAS f32x4*)(red + lane * 8), b1 = *(LAS f32x4*)(red + lane * 8 + 4);
#pragma unroll
    for (int w = 1; w < 4; ++w) { b0 += *(LAS f32x4*)(red + w * 512 + lane * 8); b1 += *(LAS f32x4*)(red + w * 512 + lane * 8 + 4); }
    float h[8], hn[8];
#pragma unroll
    for (int k = 0; k < 2; ++k) { h[2 * k] = rbf(bflo(xv[k]) + rbf(b0[2 * k])); h[2 * k + 1] = rbf(bfhi(xv[k]) + rbf(b0[2 * k + 1])); h[4 + 2 * k] = rbf(bflo(xv[2 + k]) + rbf(b1[2 * k])); h[5 + 2 * k] = rbf(bfhi(xv[2 + k]) + rbf(b1[2 * k + 1])); }
    if (wave == 3 && bi < 32 && (lane >> 1) == bi) __builtin_amdgcn_raw_buffer_store_b128(u32x4{nosent(pk2(h[0], h[1])), nosent(pk2(h[2], h[3])), nosent(pk2(h[4], h[5])), nosent(pk2(h[6], h[7]))}, rs, WS_H + bi * 64 + (lane & 1) * 16, 0, 16);
    ln8(h, hn);
    LAS char* vec = L + L_SCR + 8192 + wave * 1024;
    *(LAS u32x4*)(vec + lane * 16) = u32x4{pk2(hn[0], hn[1]), pk2(hn[2], hn[3]), pk2(hn[4], hn[5]), pk2(hn[6], hn[7])};
    f32x4 y0 = {0.f, 0.f, 0.f, 0.f}, y1 = {0.f, 0.f, 0.f, 0.f};
#pragma unroll
    for (int j = 0; j < 16; j += 2) { y0 = mfma(wf[j], asb8(*(LAS u32x4*)(vec + j * 64 + gq * 16)), y0); y1 = mfma(wf[j + 1], asb8(*(LAS u32x4*)(vec + j * 64 + 64 + gq * 16)), y1); }
    y0 += y1;
    float av[4];
#pragma unroll
    for (int i = 0; i < 4; ++i) { auto sw = __builtin_amdgcn_permlane32_swap(__float_as_uint(y0[i]), __float_as_uint(y0[i]), false, false);
      const float gt = rbf(__uint_as_float(sw[0])), up = rbf(__uint_as_float(sw[1])); av[i] = (gt > 0.f ? gt : gt * 0.f) * up; }
    LAS char* ab = L + L_SCR + 12288;
    if (n16 == 0 && gq < 2) *(LAS u32x2*)(ab + wave * 16 + gq * 8) = u32x2{nosent(pk2(av[0], av[1])), nosent(pk2(av[2], av[3]))};
    __syncthreads();
    if (tid < 4) __builtin_amdgcn_raw_buffer_store_b128(*(LAS u32x4*)(ab + tid * 16), rs, WS_ACT + bi * 64 + tid * 16, 0, 16);
    ST(8);
    return;
  }

  {
    LAS char* sl = L + wave * 16384;
    const char* src = (const char*)wd + (size_t)(448 * wave + (lane >> 1)) * 1024 + ci * 32 + (lane & 1) * 16;
#pragma unroll
    for (int i = 0; i < 14; ++i) GLDS(src + i * 32768, sl + i * 1024, 0);
    check_tags();
    waitvm<0>();
    __syncthreads();
#pragma unroll
    for (int j = 0; j < 14; ++j) { LAS char* a = sl + (32 * j + 8 * gq + q4) * 32 + p4 * 8; wf[j] = tr2(a, a + 128); }
    ST(6);
    const int po = lane < 56 ? WS_ACT + wave * 896 + lane * 16 : WS_H + ci * 64 + (lane & 1) * 16;
    u32x4 av;
#pragma unroll 1
    for (int sp = 0; sp < SPIN_G; ++sp) {
      asm volatile("" ::: "memory");
      av = __builtin_amdgcn_raw_buffer_load_b128(rs, po, 0, 16);
      if (__all(ok4(av))) break;
    }
    ST(7);
    LAS char* vec = L + L_SCR + wave * 1024;
    *(LAS u32x4*)(vec + lane * 16) = av;
    f32x4 y0 = {0.f, 0.f, 0.f, 0.f}, y1 = {0.f, 0.f, 0.f, 0.f};
#pragma unroll
    for (int j = 0; j < 14; j += 2) { y0 = mfma(wf[j], asb8(*(LAS u32x4*)(vec + j * 64 + gq * 16)), y0); y1 = mfma(wf[j + 1], asb8(*(LAS u32x4*)(vec + j * 64 + 64 + gq * 16)), y1); }
    y0 += y1;
    LAS float* red = (LAS float*)(L + L_SCR + 4096);
    if (n16 == 0) *(LAS f32x4*)(red + wave * 16 + gq * 4) = y0;
    __syncthreads();
    if (tid < 16) {
      const float yv = (red[tid] + red[16 + tid]) + (red[32 + tid] + red[48 + tid]);
      const float hv = bflo(*(LAS unsigned short*)(L + L_SCR + 896 + tid * 2));
      out[ci * 16 + tid] = (bf16_t)(pk2(hv + rbf(yv), 0.f) & 0xffffu);
    }
    if (ci == 0 && tid == 0) __hip_atomic_store((GAS u64*)(ws + WS_E), epoch + 1, RLX_AGENT);
    ST(8);
  }
}

extern "C" void kernel_launch(void* const* d_in, const int* in_sizes, int n_in, void* d_out, int out_size, void* d_ws, size_t ws_size, hipStream_t stream) {
  fused_decode<<<NWG, NTHR, 0, stream>>>((const bf16_t*)d_in[0], (const bf16_t*)d_in[1], (const bf16_t*)d_in[2], (const bf16_t*)d_in[3], (const bf16_t*)d_in[4],
                                         (const bf16_t*)d_in[6], (const bf16_t*)d_in[7], (const bf16_t*)d_in[8], (bf16_t*)d_out, (char*)d_ws);
}
```

```cpp
#include <hip/hip_runtime.h>
#include <cstdint>

typedef unsigned short bf16_t;
typedef __attribute__((ext_vector_type(2))) __bf16 bf16x2;
typedef __attribute__((ext_vector_type(4))) __bf16 bf16x4;
typedef __attribute__((ext_vector_type(8))) __bf16 bf16x8;
typedef __attribute__((ext_vector_type(2))) unsigned u32x2;
typedef __attribute__((ext_vector_type(4))) unsigned u32x4;
typedef __attribute__((ext_vector_type(2))) float f32x2;
typedef __attribute__((ext_vector_type(4))) float f32x4;
typedef unsigned long long u64;
#define GAS __attribute__((address_space(1)))
#define LAS __attribute__((address_space(3)))

#define WIN 24
#define WMAX 48
#define WSH 12
#define TAU 556
#define PLEAD -3
#define QDELAY 600
#define WWIN 56
#define WEARLY 0
#define LIMA 32
#define LIMB 2
#define LIMH 0
#define ADELAY 12
#define SYNCT 1
#define AEARLY 1

constexpr int SK = 65536, F = 1792;
constexpr int NWG = 256, NTHR = 384;
constexpr int EB = 3, NTB = 64 - EB, NTA = 65;
constexpr int PACE = TAU * 256 / 20;
constexpr unsigned SENT = 0xFFFFFFFFu;
constexpr int WS_E = 0, WS_GF = 1024, WS_QF = 4096, WS_QP = 65536, WS_P1 = 131072, P1_REC = 1152, WS_Y = WS_P1 + NWG * P1_REC, WS_Z = WS_Y + 16384, WS_END = WS_Z + 57 * 2048;
constexpr int WS_TS = 1 << 20;
constexpr int L_CTRL = 131072, L_Q = L_CTRL + 256, L_QP = L_Q + 1024, L_XN = L_QP + 1024, L_MRG = L_XN + 2048, MRG_REC = 132 * 4, L_PB = L_MRG + 16 * MRG_REC, L_RED = L_PB + 4096,
              L_VEC = L_RED + 1024, L_ACT = L_VEC + 1024, L_ZERO = L_ACT + 64, L_TS = L_ZERO + 256, L_END = L_TS + 1536;
constexpr int C_CONS = 0, C_LANDED = 16, C_QSYNC = 20, C_QRDY = 24, C_MSYNC = 28, C_STEP = 32, C_WLAND = 36, C_SHIFT = 40, C_EPOCH = 128;
constexpr float LOG2E = 1.4426950408889634f;
constexpr int SPIN_G = 400000, SPIN_L = 8000000;

__device__ __forceinline__ float fmx(float a, float b) { return __builtin_elementwise_maximum(a, b); }
__device__ __forceinline__ float bflo(unsigned u) { return __builtin_bit_cast(float, u << 16); }
__device__ __forceinline__ float bfhi(unsigned u) { return __builtin_bit_cast(float, u & 0xffff0000u); }
__device__ __forceinline__ f32x2 unpk(unsigned u) { return f32x2{bflo(u), bfhi(u)}; }
__device__ __forceinline__ unsigned pk2(f32x2 v) { return __builtin_bit_cast(unsigned, __builtin_convertvector(v, bf16x2)); }
__device__ __forceinline__ unsigned pk2(float lo, float hi) { return pk2(f32x2{lo, hi}); }
__device__ __forceinline__ float rbf(float f) { return (float)(__bf16)f; }
__device__ __forceinline__ unsigned uns(unsigned u) { return min(u, 0xFFFFFFFEu); }
__device__ __forceinline__ unsigned fns(float f) { return uns(__builtin_bit_cast(unsigned, f)); }
__device__ __forceinline__ u32x4 fns4(f32x4 v) { return u32x4{fns(v[0]), fns(v[1]), fns(v[2]), fns(v[3])}; }
__device__ __forceinline__ u32x2 pk4s(f32x4 v) { return u32x2{uns(pk2(v[0], v[1])), uns(pk2(v[2], v[3]))}; }
__device__ __forceinline__ unsigned umax3(unsigned a, unsigned b, unsigned c) { return max(max(a, b), c); }
template <int CTRL> __device__ __forceinline__ float dpp(float x) { return __builtin_bit_cast(float, __builtin_amdgcn_mov_dpp(__builtin_bit_cast(int, x), CTRL, 0xf, 0xf, true)); }
constexpr int XOR1 = 0xB1, XOR2 = 0x4E, HMIR = 0x141, ROR8 = 0x128;
__device__ __forceinline__ float x16_max(float x) { auto s = __builtin_amdgcn_permlane16_swap(__float_as_uint(x), __float_as_uint(x), false, false); return fmx(__uint_as_float(s[0]), __uint_as_float(s[1])); }
__device__ __forceinline__ float x32_max(float x) { auto s = __builtin_amdgcn_permlane32_swap(__float_as_uint(x), __float_as_uint(x), false, false); return fmx(__uint_as_float(s[0]), __uint_as_float(s[1])); }
__device__ __forceinline__ float x16_sum(float x) { auto s = __builtin_amdgcn_permlane16_swap(__float_as_uint(x), __float_as_uint(x), false, false); return __uint_as_float(s[0]) + __uint_as_float(s[1]); }
__device__ __forceinline__ float x32_sum(float x) { auto s = __builtin_amdgcn_permlane32_swap(__float_as_uint(x), __float_as_uint(x), false, false); return __uint_as_float(s[0]) + __uint_as_float(s[1]); }
__device__ __forceinline__ float row_sum(float x) { x += dpp<XOR1>(x); x += dpp<XOR2>(x); x += dpp<HMIR>(x); x += dpp<ROR8>(x); return x; }
__device__ __forceinline__ float row_max(float x) { x = fmx(x, dpp<XOR1>(x)); x = fmx(x, dpp<XOR2>(x)); x = fmx(x, dpp<HMIR>(x)); x = fmx(x, dpp<ROR8>(x)); return x; }
__device__ __forceinline__ float wave_sum(float x) { return x32_sum(x16_sum(row_sum(x))); }
__device__ __forceinline__ float dot2(unsigned a, unsigned b, float c) { return __builtin_amdgcn_fdot2_f32_bf16(__builtin_bit_cast(bf16x2, a), __builtin_bit_cast(bf16x2, b), c, false); }
template <int N> __device__ __forceinline__ void waitvm() { asm volatile("s_waitcnt vmcnt(%0)" :: "i"(N) : "memory"); }
__device__ __forceinline__ void waitlgkm() { asm volatile("s_waitcnt lgkmcnt(0)" ::: "memory"); }
__device__ __forceinline__ void wgbar() { asm volatile("s_waitcnt lgkmcnt(0)\n\ts_barrier" ::: "memory"); }
__device__ __forceinline__ void lds_st(unsigned addr, unsigned v) { asm volatile("ds_write_b32 %0, %1" :: "v"(addr), "v"(v) : "memory"); }
__device__ __forceinline__ unsigned lds_ld(unsigned addr) { unsigned v; asm volatile("ds_read_b32 %0, %1\n\ts_waitcnt lgkmcnt(0)" : "=&v"(v) : "v"(addr) : "memory"); return __builtin_amdgcn_readfirstlane(v); }
__device__ __forceinline__ void lds_wait_ge(unsigned addr, unsigned want) {
#pragma unroll 1
  for (int sp = 0; sp < SPIN_L; ++sp) { if (lds_ld(addr) >= want) break; __builtin_amdgcn_s_sleep(1); } }
__device__ __forceinline__ void lds_spin_ge(unsigned addr, unsigned want) {
#pragma unroll 1
  for (int sp = 0; sp < 2 * SPIN_L; ++sp) { if (lds_ld(addr) >= want) break; } }
__device__ __forceinline__ int vm_count() { unsigned ib; asm volatile("s_getreg_b32 %0, hwreg(HW_REG_IB_STS)" : "=s"(ib) :: "memory"); return (ib & 15) | ((ib >> 18) & 48); }
__device__ __forceinline__ bf16x8 asb8(u32x4 v) { return __builtin_bit_cast(bf16x8, v); }
__device__ __forceinline__ f32x4 asf4(u32x4 v) { return __builtin_bit_cast(f32x4, v); }
__device__ __forceinline__ f32x2 asf2(u32x2 v) { return __builtin_bit_cast(f32x2, v); }
__device__ __forceinline__ f32x4 mfma(bf16x8 a, bf16x8 b, f32x4 c) { return __builtin_amdgcn_mfma_f32_16x16x32_bf16(a, b, c, 0, 0, 0); }
__device__ __forceinline__ bf16x8 tr2(LAS char* p0, LAS char* p1) {
  bf16x4 a = __builtin_amdgcn_ds_read_tr16_b64_v4bf16((LAS bf16x4*)p0), b = __builtin_amdgcn_ds_read_tr16_b64_v4bf16((LAS bf16x4*)p1);
  return __builtin_shufflevector(a, b, 0, 1, 2, 3, 4, 5, 6, 7);
}
#define GLDS(src, dst, off) __builtin_amdgcn_global_load_lds((const GAS unsigned*)(src), (LAS unsigned*)(dst), 16, (off), 2)
#define PIN8(a) asm volatile("s_waitcnt lgkmcnt(0)" : "+v"(a[0]), "+v"(a[1]), "+v"(a[2]), "+v"(a[3]), "+v"(a[4]), "+v"(a[5]), "+v"(a[6]), "+v"(a[7]) :: "memory")
#define BLD32(off) __builtin_amdgcn_raw_buffer_load_b32(rs, (off), 0, 16)
#define BLD64(off) __builtin_amdgcn_raw_buffer_load_b64(rs, (off), 0, 16)
#define BLD128(off) __builtin_amdgcn_raw_buffer_load_b128(rs, (off), 0, 16)
#define BST32(v, off) __builtin_amdgcn_raw_buffer_store_b32((v), rs, (off), 0, 16)
#define BST64(v, off) __builtin_amdgcn_raw_buffer_store_b64((v), rs, (off), 0, 16)
#define BST128(v, off) __builtin_amdgcn_raw_buffer_store_b128((v), rs, (off), 0, 16)
#define ST(i) do {} while (0)
#define CY(i) do {} while (0)
#define CYOUT(n) do {} while (0)

__device__ __forceinline__ bf16x8 softmax_tile(const bf16x8 (&kf)[8], const bf16x8 (&qf)[4], float& m, float& l, float& alpha) {
  f32x4 s0 = {0.f, 0.f, 0.f, 0.f}, s1 = {0.f, 0.f, 0.f, 0.f};
#pragma unroll
  for (int j = 0; j < 4; ++j) { s0 = mfma(kf[2 * j], qf[j], s0); s1 = mfma(kf[2 * j + 1], qf[j], s1); }
  f32x2 sc[4];
  constexpr float SCALE = 0.08838834764831845f;
  sc[0] = unpk(pk2(unpk(pk2(s0[0], s0[1])) * SCALE)); sc[1] = unpk(pk2(unpk(pk2(s0[2], s0[3])) * SCALE));
  sc[2] = unpk(pk2(unpk(pk2(s1[0], s1[1])) * SCALE)); sc[3] = unpk(pk2(unpk(pk2(s1[2], s1[3])) * SCALE));
  float tm = fmx(fmx(fmx(sc[0][0], sc[0][1]), fmx(sc[1][0], sc[1][1])), fmx(fmx(sc[2][0], sc[2][1]), fmx(sc[3][0], sc[3][1])));
  tm = x32_max(x16_max(tm));
  const float mn = fmx(m, tm);
  alpha = __builtin_amdgcn_exp2f((m - mn) * LOG2E);
  f32x2 p[4];
#pragma unroll
  for (int i = 0; i < 4; ++i) { const f32x2 d = (sc[i] - mn) * LOG2E; p[i] = f32x2{__builtin_amdgcn_exp2f(d[0]), __builtin_amdgcn_exp2f(d[1])}; }
  const f32x2 ps = (p[0] + p[1]) + (p[2] + p[3]);
  l = fmaf(l, alpha, ps[0] + ps[1]); m = mn;
  return asb8(u32x4{pk2(p[0]), pk2(p[1]), pk2(p[2]), pk2(p[3])});
}

__global__ void __launch_bounds__(NTHR, 1) fused_decode(const bf16_t* __restrict__ x, const bf16_t* __restrict__ kc, const bf16_t* __restrict__ vc, const bf16_t* __restrict__ wq,
                                                        const bf16_t* __restrict__ wo, const bf16_t* __restrict__ wg, const bf16_t* __restrict__ wu, const bf16_t* __restrict__ wd,
                                                        bf16_t* __restrict__ out, char* ws) {
  __shared__ __attribute__((aligned(1024))) char lds_raw[L_END];
  const u64 tk = __builtin_amdgcn_s_memrealtime();
  LAS char* L = (LAS char*)lds_raw;
  const unsigned lc = (unsigned)(size_t)L + L_CTRL;
  const int tid = threadIdx.x, lane = tid & 63, wave = __builtin_amdgcn_readfirstlane(tid >> 6), bid = blockIdx.x;
  const int g = bid & 7, s = bid >> 3, rec = g * 32 + s;
  const bool roleA = s < 16, roleB = s >= 16 && s < 23, roleC = s >= 28;
  const int bi = g * 7 + (s - 16), ci = g * 4 + (s - 28);
  const int NT = roleB ? NTB : roleC ? 64 : 65, N1 = NT - 4;
  const int xe = s < 16 ? s : s - 7;
  const int x0 = NT == 65 ? (16 + xe / EB - s) * 64 + NTB + xe % EB : NT - 1;
  const unsigned off_e = (lane >> 4) * 256 + (((lane & 15) ^ ((lane >> 4) << 1)) << 4), off_o = off_e ^ 128;
  const char* ks = (const char*)kc + (size_t)rec * (2048 * 256);
  const char* vs = (const char*)vc + (size_t)rec * (2048 * 256);
  const __amdgpu_buffer_rsrc_t rs = __builtin_amdgcn_make_buffer_rsrc(ws, 0, WS_END, 0x00020000);
  if (wave == 4) {
    __builtin_amdgcn_s_setprio(3);
#pragma unroll
    for (int q = 0; q < 8; ++q) {
      const char* p = ((q & 2) ? vs : ks) + (q & 1) * 4096 + (q < 4 ? (long)x0 * 8192 : 0l);
      GLDS(p + off_e, L + q * 4096, 0); GLDS(p + off_o, L + q * 4096, 1024); GLDS(p + off_e, L + q * 4096, 2048); GLDS(p + off_o, L + q * 4096, 3072);
    }
  } else if (wave < 4) {
    const u32x4 sv = {SENT, SENT, SENT, SENT};
    BST128(sv, tid < 72 ? WS_P1 + rec * P1_REC + tid * 16 : (roleA && tid >= 248) ? WS_Y + (g * 16 + s) * 128 + (tid - 248) * 16 : WS_END);
    if (roleB) BST128(sv, (tid >= 128 || bi == 0) ? WS_Z + (tid >= 128 ? bi : 56) * 2048 + (tid & 127) * 16 : WS_END);
    if (tid < 128) lds_st(tid < 64 ? lc + tid * 4 : lc - L_CTRL + L_ZERO + (tid - 64) * 4, 0u);
  }
  asm volatile("" :: "s"(x), "s"(wq), "s"(ws));
  wgbar();

  if (wave == 4) {
    const unsigned la = lc + C_LANDED;
    unsigned due = ((unsigned)tk << 8) + (4 - PLEAD) * PACE;
    auto pace = [&](int issued) {
      unsigned a, b, c, v;
      asm volatile("1:\n\ts_memrealtime s[90:91]\n\ts_getreg_b32 %0, hwreg(HW_REG_IB_STS)\n\ts_bfe_u32 %1, %0, 0x20016\n\ts_and_b32 %0, %0, 15\n\ts_lshl4_add_u32 %0, %1, %0\n\t"
                   "s_sub_i32 %1, %5, %0\n\tv_mov_b32 %3, %1\n\tds_write_b32 %4, %3\n\ts_waitcnt lgkmcnt(0)\n\ts_lshl_b32 %2, s90, 8\n\ts_sub_u32 %1, %6, %2\n\t"
                   "s_cmp_lt_u32 %1, %7\n\ts_cbranch_scc1 1b\n\t"
                   "s_sub_u32 %2, %2, %6\n\ts_lshr_b32 %2, %2, %8\n\ts_min_u32 %2, %2, %9\n\ts_add_u32 %2, %2, %10\n\t"
                   "s_cmp_gt_u32 %0, %2\n\ts_cbranch_scc1 1b"
                   : "=&s"(a), "=&s"(b), "=&s"(c), "=&v"(v) : "v"(la), "s"(issued), "s"(due), "s"(64 * PACE), "i"(WSH), "i"(WMAX - WIN), "i"(WIN - 8) : "memory", "scc", "s90", "s91");
      due += PACE;
    };
    auto wait_slot = [&](int u, int issued) {
#pragma unroll 1
      for (int sp = 0; sp < SPIN_L; ++sp) {
        if (__builtin_expect(lds_ld(lc + C_CONS + (u & 3) * 4) >= (unsigned)(u - 7), 1)) break;
        lds_st(la, issued - vm_count());
      }
    };
#pragma unroll 1
    for (int u = 2; u < N1; ++u) {
      if (u >= 8) wait_slot(u, 16 * u);
      if (u == 20) due += lds_ld(lc + C_SHIFT);
      LAS char* dst = L + (u & 7) * 16384;
#pragma unroll
      for (int q = 0; q < 4; ++q) {
        if ((q & 1) == 0) pace(16 * u + 4 * q);
        const char* p = ((q & 2) ? vs : ks) + (size_t)(u - 1) * 8192 + (q & 1) * 4096;
        GLDS(p + off_e, dst + q * 4096, 0); GLDS(p + off_o, dst + q * 4096, 1024); GLDS(p + off_e, dst + q * 4096, 2048); GLDS(p + off_o, dst + q * 4096, 3072);
      }
    }
#pragma unroll 1
    for (int h = 0; h < 8; ++h) {
      const int u = N1 + (h & 3);
      if (h < 4) wait_slot(u, 16 * N1 + 8 * h);
      LAS char* dst = L + (u & 7) * 16384 + (h >> 2) * 8192;
      const char* p = ((h & 4) ? vs : ks) + (size_t)(u - 1) * 8192;
      pace(16 * N1 + 8 * h);
      GLDS(p + off_e, dst, 0); GLDS(p + off_o, dst, 1024); GLDS(p + off_e, dst, 2048); GLDS(p + off_o, dst, 3072);
      GLDS(p + 4096 + off_e, dst + 4096, 0); GLDS(p + 4096 + off_o, dst + 4096, 1024); GLDS(p + 4096 + off_e, dst + 4096, 2048); GLDS(p + 4096 + off_o, dst + 4096, 3072);
      lds_st(lc + C_STEP, h + 1);
    }
    waitvm<24>(); lds_st(la, 16 * NT - 24);
    waitvm<16>(); lds_st(la, 16 * NT - 16);
    waitvm<8>(); lds_st(la, 16 * NT - 8);
    waitvm<0>(); lds_st(la, 16 * NT);
    waitlgkm();
    ST(2);
    return;
  }

  if (wave == 5) {
    const u32x2 ev = BLD64(WS_E);
    const u64 epoch = ((u64)(unsigned)__builtin_amdgcn_readfirstlane(ev[1]) << 32) | (unsigned)__builtin_amdgcn_readfirstlane(ev[0]);
    u64 z = epoch + 0x9E3779B97F4A7C15ull; z = (z ^ (z >> 30)) * 0xBF58476D1CE4E5B9ull; z = (z ^ (z >> 27)) * 0x94D049BB133111EBull; z ^= z >> 31;
    const unsigned thi = (unsigned)(z >> 32), tlo = (unsigned)z;
    if (lane == 0) *(LAS u32x2*)(L + L_CTRL + C_EPOCH) = u32x2{(unsigned)(epoch + 1), (unsigned)((epoch + 1) >> 32)};
    lds_wait_ge(lc + C_QSYNC, 4u);
    LAS float* qp = (LAS float*)(L + L_QP);
    BST32(__float_as_uint((qp[lane] + qp[64 + lane]) + (qp[128 + lane] + qp[192 + lane])), WS_QP + rec * 256 + lane * 4);
    waitvm<0>();
    if (lane < 2) BST64((lane ? u32x2{(unsigned)tk, tlo} : u32x2{tlo, thi}), WS_QF + rec * 128 + lane * 8);
    ST(2);
    while (__builtin_amdgcn_s_memrealtime() - tk < QDELAY) __builtin_amdgcn_s_sleep(4);
#pragma unroll 1
    for (int sp = 0; sp < SPIN_G; ++sp) {
      asm volatile("" ::: "memory");
      const u32x2 t = BLD64(lane < 32 ? WS_QF + (g * 32 + lane) * 128 : WS_QF + ((lane & 7) * 32 + s) * 128 + 8);
      if (((unsigned)__ballot((t[0] == tlo) & (t[1] == thi))) == 0xffffffffu) {
        int d = (lane >= 32 && t[1] == tlo) ? (int)(t[0] - (unsigned)tk) : 0;
        d = min(d, __builtin_amdgcn_mov_dpp(d, XOR1, 0xf, 0xf, true)); d = min(d, __builtin_amdgcn_mov_dpp(d, XOR2, 0xf, 0xf, true)); d = min(d, __builtin_amdgcn_mov_dpp(d, HMIR, 0xf, 0xf, true));
        lds_st(lc + C_SHIFT, max(__builtin_amdgcn_readlane(d, 32), -200) * 256);
        break;
      }
      __builtin_amdgcn_s_sleep(16);
    }
    asm volatile("" ::: "memory");
    {
      f32x4 a[4], b[4];
#pragma unroll
      for (int i = 0; i < 4; ++i) { const int o = WS_QP + (g * 32 + (lane >> 3) * 4 + i) * 256 + (lane & 7) * 32; a[i] = asf4(BLD128(o)); b[i] = asf4(BLD128(o + 16)); }
      const f32x4 sa = (a[0] + a[1]) + (a[2] + a[3]), sb = (b[0] + b[1]) + (b[2] + b[3]);
      *(LAS u32x4*)(L + L_Q + lane * 16) = u32x4{pk2(sa[0], sa[1]), pk2(sa[2], sa[3]), pk2(sb[0], sb[1]), pk2(sb[2], sb[3])};
    }
    waitlgkm();
    lds_st(lc + C_QRDY, 1u);
    ST(3);
    if (s == 23 && lane == 0) BST64((u32x2{tlo, thi}), WS_GF + g * 128);
    if (roleB || roleC) {
      while (__builtin_amdgcn_s_memrealtime() - tk < 2000) __builtin_amdgcn_s_sleep(32);
#pragma unroll 1
      for (int sp = 0; sp < SPIN_G; ++sp) {
        asm volatile("" ::: "memory");
        const u32x2 t = BLD64(WS_GF + (lane & 7) * 128);
        if (__all((t[0] == tlo) & (t[1] == thi))) break;
        __builtin_amdgcn_s_sleep(32);
      }
    }
    lds_st(lc + C_QRDY, 2u);
    __builtin_amdgcn_s_waitcnt(0);
    bool p1out = false;
    auto check_p1 = [&]() { unsigned v; asm volatile("ds_read_b32 %0, %1\n\ts_waitcnt lgkmcnt(0)" : "=&v"(v) : "v"(lc + C_CONS + (lane & 3) * 4) : "memory"); p1out = __all(v > (unsigned)NT); };
    auto hold = [&](int lim) {
#pragma unroll 1
      for (int sp = 0; sp < SPIN_L && !p1out && vm_count() > lim; ++sp) check_p1();
    };
    auto wunit = [&](int lim, int kw, const char* p, unsigned o0, unsigned o1, unsigned o2, unsigned o3, int stride4, unsigned x8) {
      const unsigned ca = lc + C_CONS + ((kw < 4 ? NT + kw : lane) & 3) * 4;
#pragma unroll 1
      for (int sp = 0; sp < SPIN_L; ++sp) {
        unsigned v; asm volatile("ds_read_b32 %0, %1\n\ts_waitcnt lgkmcnt(0)" : "=&v"(v) : "v"(ca) : "memory");
        if (__all(v >= (unsigned)(NT - 7 + kw))) break;
      }
      LAS char* dst = L + ((NT + kw) & 7) * 16384;
#pragma unroll
      for (int q = 0; q < 4; ++q) {
        hold(lim);
        waitvm<WWIN - 4>();
        if (q == 2) { o0 ^= x8; o1 ^= x8; o2 ^= x8; o3 ^= x8; }
        GLDS(p + o0, dst + q * 4096, 0); GLDS(p + o1, dst + q * 4096, 1024); GLDS(p + o2, dst + q * 4096, 2048); GLDS(p + o3, dst + q * 4096, 3072);
        p += stride4;
      }
    };
    constexpr int RS = F * 2;
    asm volatile("" :: "s"(wo), "s"(wg), "s"(wu), "s"(wd));
    if (roleA) {
      const unsigned lo = (lane >> 3) * 1024 + (((lane & 7) * 16) ^ (((lane >> 4) & 1) << 5));
      const char* p = (const char*)wo + (size_t)((4 * g + 2 * (s >> 3)) * 128) * 1024 + (s & 7) * 128;
      lds_wait_ge(lc + C_STEP, 8 - AEARLY);
#pragma unroll 1
      for (int kw = 0; kw < 2; ++kw) wunit(LIMA, kw, p + kw * 131072, lo, (lo ^ 64) + 7168, lo + 2 * 7168, (lo ^ 64) + 3 * 7168, 32768, 0u);
    } else if (roleB) {
      constexpr int D1 = 16 * RS - 1024;
      lds_wait_ge(lc + C_STEP, 8 - WEARLY);
#pragma unroll 1
      for (int kw = 0; kw < 4; ++kw) {
        const unsigned lo = (lane >> 2) * RS + (((lane & 3) ^ ((lane >> 5) | (kw & 2))) << 4);
        wunit(LIMB, kw, (const char*)((kw >> 1) ? wu : wg) + (size_t)((kw & 1) * 256) * RS + bi * 64, lo, lo + D1, lo + 2 * D1, lo + 3 * D1, 64 * RS, 0u);
      }
      const unsigned lo = lane * 16;
#pragma unroll 1
      for (int kw = 0; kw < 2; ++kw) wunit(LIMB, 4 + kw, (const char*)wd + (size_t)(bi * 32 + kw * 16) * 1024, lo, lo ^ 32, lo ^ 64, lo ^ 96, 4096, 128u);
      waitvm<32>(); lds_st(lc + C_WLAND, 1u);
    }
    waitvm<0>(); lds_st(lc + C_WLAND, 2u);
    ST(4);
    __builtin_amdgcn_s_waitcnt(0);
    return;
  }

  const int n16 = lane & 15, gq = lane >> 4, q4 = n16 >> 2, p4 = lane & 3;
  {
    u32x4 xv = *(const u32x4*)(x + lane * 8);
    u32x4 w[4];
    const int r0 = (s & 3) * 128 + (tid >> 3);
#pragma unroll
    for (int i = 0; i < 4; ++i) w[i] = __builtin_nontemporal_load((const u32x4*)(wq + (size_t)(r0 + 32 * i) * 4096 + g * 512 + (s >> 2) * 64 + (tid & 7) * 8));
    asm volatile("" : "+v"(xv) :: "memory");
    float sx = 0.f;
#pragma unroll
    for (int k = 0; k < 4; ++k) sx = dot2(xv[k], 0x3f803f80u, sx);
    const float mean = wave_sum(sx) * (1.f / 512.f);
    f32x2 d[4]; float sq = 0.f;
#pragma unroll
    for (int k = 0; k < 4; ++k) { d[k] = unpk(xv[k]); const f32x2 e = d[k] - mean; sq = fmaf(e[0], e[0], fmaf(e[1], e[1], sq)); }
    const float var = wave_sum(sq) * (1.f / 512.f);
    const float mb = rbf(mean), rsd = __builtin_amdgcn_rcpf(rbf(__builtin_amdgcn_sqrtf(rbf(rbf(var) + 1e-5f))));
#pragma unroll
    for (int k = 0; k < 4; ++k) d[k] = unpk(pk2(unpk(pk2(d[k] - mb)) * rsd));
    LAS float* xs = (LAS float*)(L + L_XN);
    *(LAS f32x4*)(xs + lane * 8) = f32x4{d[0][0], d[0][1], d[1][0], d[1][1]};
    *(LAS f32x4*)(xs + lane * 8 + 4) = f32x4{d[2][0], d[2][1], d[3][0], d[3][1]};
    float xr[4];
#pragma unroll
    for (int i = 0; i < 4; ++i) xr[i] = xs[r0 + 32 * i];
    asm volatile("" : "+v"(xr[0]), "+v"(xr[1]), "+v"(xr[2]), "+v"(xr[3]), "+v"(w[0]));
    f32x2 acc[4];
#pragma unroll
    for (int k = 0; k < 4; ++k) acc[k] = unpk(w[0][k]) * xr[0];
#pragma unroll
    for (int i = 1; i < 4; ++i)
#pragma unroll
      for (int k = 0; k < 4; ++k) acc[k] = unpk(w[i][k]) * xr[i] + acc[k];
    float a[8];
#pragma unroll
    for (int k = 0; k < 8; ++k) { float v = acc[k >> 1][k & 1]; v += dpp<ROR8>(v); a[k] = x32_sum(x16_sum(v)); }
    LAS float* qp = (LAS float*)(L + L_QP);
    if (lane < 8) { *(LAS f32x4*)(qp + wave * 64 + lane * 8) = f32x4{a[0], a[1], a[2], a[3]}; *(LAS f32x4*)(qp + wave * 64 + lane * 8 + 4) = f32x4{a[4], a[5], a[6], a[7]}; }
    asm volatile("s_waitcnt vmcnt(0) lgkmcnt(0)" ::: "memory");
    if (lane == 0) __hip_atomic_fetch_add((LAS unsigned*)(L + L_CTRL + C_QSYNC), 1u, __ATOMIC_RELAXED, __HIP_MEMORY_SCOPE_WORKGROUP);
    ST(1);
  }
  unsigned koff[4], voff[8];
#pragma unroll
  for (int j = 0; j < 4; ++j) koff[j] = n16 * 256 + (((gq ^ ((lane & 7) << 1)) ^ (4 * j)) << 4);
  const int r7 = 4 * (gq & 1) + q4;
#pragma unroll
  for (int c = 0; c < 8; ++c) voff[c] = 8192 + (4 * gq + q4) * 256 + ((c ^ r7) << 5) + p4 * 8;
  const unsigned cfree = lc + C_CONS + wave * 4;
  auto read_k = [&](int t, bf16x8 (&kf)[8]) {
    LAS char* sl = L + (t & 7) * 16384;
#pragma unroll
    for (int j = 0; j < 4; ++j) { kf[2 * j] = asb8(*(LAS u32x4*)(sl + koff[j])); kf[2 * j + 1] = asb8(*(LAS u32x4*)(sl + koff[j] + 4096)); }
    __builtin_amdgcn_sched_barrier(0);
  };
  auto read_v = [&](int t, bf16x8 (&vf)[8]) {
    LAS char* sl = L + (t & 7) * 16384;
#pragma unroll
    for (int c = 0; c < 8; ++c) vf[c] = tr2(sl + voff[c], sl + voff[c] + 4096);
  };
  {
    bf16x8 qf[4];
    float m = -3.0e38f, l = 0.f, alpha;
    f32x4 acc[8];
    {
      bf16x8 ka[8], va[8], kb[8], vb[8];
      lds_wait_ge(lc + C_LANDED, 16 * wave + 16);
      read_k(wave, ka); read_v(wave, va); PIN8(ka); PIN8(va);
      lds_st(cfree, wave + 1);
      lds_wait_ge(lc + C_LANDED, 16 * wave + 80);
      read_k(wave + 4, kb); read_v(wave + 4, vb); PIN8(kb); PIN8(vb);
      lds_st(cfree, wave + 5);
      lds_wait_ge(lc + C_QRDY, 1u);
#pragma unroll
      for (int j = 0; j < 4; ++j) qf[j] = asb8(*(LAS u32x4*)(L + L_Q + (n16 & 3) * 256 + j * 64 + gq * 16));
      ST(3);
      bf16x8 pf = softmax_tile(ka, qf, m, l, alpha);
#pragma unroll
      for (int c = 0; c < 8; ++c) acc[c] = mfma(va[c], pf, f32x4{0.f, 0.f, 0.f, 0.f});
      pf = softmax_tile(kb, qf, m, l, alpha);
#pragma unroll
      for (int c = 0; c < 8; ++c) acc[c] = mfma(vb[c], pf, acc[c] * alpha);
    }
#pragma unroll 1
    for (int t = wave + 8; t < N1; t += 4) {
      bf16x8 kf[8], vf[8];
      lds_wait_ge(lc + C_LANDED, 16 * t + 8);
      read_k(t, kf);
      const bf16x8 pf = softmax_tile(kf, qf, m, l, alpha);
#pragma unroll
      for (int c = 0; c < 8; ++c) acc[c] *= alpha;
      asm volatile("" : "+v"(acc[0]), "+v"(acc[1]), "+v"(acc[2]), "+v"(acc[3]), "+v"(acc[4]), "+v"(acc[5]), "+v"(acc[6]), "+v"(acc[7]));
      lds_wait_ge(lc + C_LANDED, 16 * t + 16);
      read_v(t, vf); PIN8(vf);
      lds_st(cfree, t + 1);
#pragma unroll
      for (int c = 0; c < 8; ++c) acc[c] = mfma(vf[c], pf, acc[c]);
    }
    LAS float* r = (LAS float*)(L + L_MRG + (((wave - N1) & 3) * 4 + (n16 & 3)) * MRG_REC);
    if (n16 < 4) {
#pragma unroll
      for (int c = 0; c < 8; ++c) *(LAS f32x4*)(r + 4 + 16 * c + 4 * gq) = acc[c];
    }
    bf16x8 kf[8];
    const int jw = (wave - N1) & 3;
    lds_spin_ge(lc + C_LANDED, 16 * N1 + 8 * jw + 8);
    read_k(N1 + jw, kf);
    const bf16x8 pf = softmax_tile(kf, qf, m, l, alpha);
    *(LAS bf16x8*)(L + L_PB + jw * 1024 + lane * 16) = pf;
    l = x32_sum(x16_sum(l));
    *(LAS f32x4*)r = f32x4{m, l, alpha, 0.f};
    waitlgkm();
    if (lane == 0) __hip_atomic_fetch_add((LAS unsigned*)(L + L_CTRL + C_MSYNC), 1u, __ATOMIC_RELAXED, __HIP_MEMORY_SCOPE_WORKGROUP);
  }
  {
    unsigned sa[4], sb[4];
    int po = n16 < 8 ? WS_P1 + rec * P1_REC + wave * 256 + (n16 & 3) * 64 + (n16 & 4) * 8 + gq * 8 : WS_END;
    int lo = (wave == 0 && lane < 4) ? WS_P1 + rec * P1_REC + 1024 + lane * 4 : WS_END;
    unsigned ma = L_MRG + (n16 & 3) * MRG_REC, mo = ma + 16 + 128 * wave + (n16 & 4) * 16 + 16 * gq, paa[4], pba[4];
#pragma unroll
    for (int j = 0; j < 4; ++j) {
      paa[j] = (n16 & 4) ? L_ZERO : L_PB + j * 1024 + lane * 16; pba[j] = (n16 & 4) ? L_PB + j * 1024 + lane * 16 : L_ZERO;
      sa[j] = ((N1 + j) & 7) * 16384 + 8192 + (4 * gq + q4) * 256 + (((2 * wave) ^ r7) << 5) + p4 * 8; sb[j] = sa[j] ^ 32;
      asm volatile("" : "+v"(paa[j]), "+v"(pba[j]), "+v"(sa[j]), "+v"(sb[j])); }
    asm volatile("" : "+v"(po), "+v"(lo), "+v"(ma), "+v"(mo));
    lds_spin_ge(lc + C_MSYNC, 4u);
    f32x4 ml[4], oa[4]; bf16x8 pa[4], pb[4];
#pragma unroll
    for (int j = 0; j < 4; ++j) ml[j] = *(LAS f32x4*)(L + ma + j * 4 * MRG_REC);
#pragma unroll
    for (int j = 0; j < 4; ++j) { oa[j] = *(LAS f32x4*)(L + mo + j * 4 * MRG_REC); pa[j] = *(LAS bf16x8*)(L + paa[j]); pb[j] = *(LAS bf16x8*)(L + pba[j]); }
    __builtin_amdgcn_sched_barrier(0);
    const float M = fmx(fmx(ml[0][0], ml[1][0]), fmx(ml[2][0], ml[3][0]));
    float e[4], lt = 0.f;
#pragma unroll
    for (int j = 0; j < 4; ++j) { e[j] = __builtin_amdgcn_exp2f((ml[j][0] - M) * LOG2E); lt = fmaf(e[j], ml[j][1], lt); }
    BST32(fns(fmaf(M, LOG2E, __builtin_amdgcn_logf(lt))), lo);
    const float inv = __builtin_amdgcn_rcpf(lt);
    f32x4 o = {0.f, 0.f, 0.f, 0.f};
#pragma unroll
    for (int j = 0; j < 4; ++j) { e[j] *= inv; o += oa[j] * (e[j] * ml[j][2]); }
    ST(4);
    const f32x4 z4 = {0.f, 0.f, 0.f, 0.f};
    asm volatile("" : "+v"(o), "+v"(e[0]), "+v"(e[1]), "+v"(e[2]), "+v"(e[3]));
    lds_spin_ge(lc + C_LANDED, 16 * N1 + 48);
    {
      bf16x8 v[4];
#pragma unroll
      for (int j = 0; j < 2; ++j) { v[2 * j] = tr2(L + sa[j], L + sa[j] + 4096); v[2 * j + 1] = tr2(L + sb[j], L + sb[j] + 4096); }
      __builtin_amdgcn_sched_barrier(0);
      o += mfma(v[1], pb[0], mfma(v[0], pa[0], z4)) * e[0];
      o += mfma(v[3], pb[1], mfma(v[2], pa[1], z4)) * e[1];
    }
#pragma unroll
    for (int j = 2; j < 4; ++j) {
      asm volatile("" : "+v"(o));
      if (j == 2) lds_st(cfree, N1 + 2);
      lds_spin_ge(lc + C_LANDED, 16 * N1 + 40 + 8 * j);
      const bf16x8 v0 = tr2(L + sa[j], L + sa[j] + 4096), v1 = tr2(L + sb[j], L + sb[j] + 4096);
      o += mfma(v1, pb[j], mfma(v0, pa[j], z4)) * e[j];
    }
    BST64(pk4s(o), po);
    lds_st(cfree, NT + 1);
  }
  ST(5);

  if (roleA) {
    const int hh = 2 * (s >> 3) + (wave >> 1), kt = wave & 1;
    const int pb = WS_P1 + (g * 32 + kt * 16 + n16) * P1_REC + hh * 64 + gq * 16;
    const int lb = WS_P1 + (g * 32 + (lane & 31)) * P1_REC + 1024 + hh * 4;
    u32x4 ov[4]; bf16x8 wf[16]; f32x4 we;
#pragma unroll
    for (int i = 0; i < 16; ++i) asm volatile("" : "=v"(wf[i]));
    asm volatile("" : "=v"(ov[0]), "=v"(ov[1]), "=v"(ov[2]), "=v"(ov[3]), "=v"(we));
    int yo = tid < 16 ? WS_Y + (g * 16 + s) * 128 + tid * 8 : WS_END;
    unsigned la0 = L_RED + wave * 256 + ((gq >> 1) | ((gq & 1) << 1)) * 64 + n16 * 4, la1 = L_RED + n16 * 16, la2 = L_VEC + wave * 256 + lane * 4, la3 = L_VEC + wave * 256 + kt * 64 + gq * 16;
    unsigned wa = ((NTA + (wave >> 1)) & 7) * 16384 + (8 * gq + q4) * 128 + p4 * 8, wx = ((q4 >> 1) | ((gq & 1) << 1)) << 5;
    asm volatile("" : "+v"(yo), "+v"(la0), "+v"(la1), "+v"(la2), "+v"(la3), "+v"(wa), "+v"(wx));
    if (ADELAY) __builtin_amdgcn_s_sleep(ADELAY);
    unsigned pend = 7u;
#pragma unroll 1
    for (int sp = 0; sp < SPIN_G; ++sp) {
      unsigned lse;
      asm volatile("" : "=v"(lse) :: "memory");
      if (pend & 2u) lse = BLD32(lb);
      if (pend & 1u) {
#pragma unroll
        for (int j = 0; j < 4; ++j) ov[j] = BLD128(pb + j * 256);
      }
      if (pend & 4u) {
        bool w;
        do w = lds_ld(lc + C_WLAND) >= 2u; while (!w && vm_count() != 0);
        if (w) {
#pragma unroll
          for (int ct = 0; ct < 4; ++ct)
#pragma unroll
            for (int j = 0; j < 4; ++j) { LAS char* a = L + wa + ((ct << 5) ^ wx) + j * 4096; wf[ct * 4 + j] = tr2(a, a + 512); }
          pend &= ~4u;
          ST(6);
        }
      }
      if ((pend & 2u) && __all(lse != SENT)) {
        pend &= ~2u;
        const float ls = __uint_as_float(lse);
        const float M = x16_max(row_max(ls));
        float e = __builtin_amdgcn_exp2f(ls - M);
        e *= __builtin_amdgcn_rcpf(x16_sum(row_sum(e)));
        *(LAS float*)(L + la2) = e;
        we = *(LAS f32x4*)(L + la3);
      }
      if (pend & 1u) {
        unsigned mx = 0;
#pragma unroll
        for (int j = 0; j < 4; ++j) mx = umax3(umax3(mx, ov[j][0], ov[j][1]), ov[j][2], ov[j][3]);
        if (__all(mx != SENT)) pend &= ~1u;
      }
      if (!pend) break;
    }
    ST(7);
    CY(0);
    f32x4 dd[4];
#pragma unroll
    for (int ct = 0; ct < 4; ++ct) {
      dd[ct] = f32x4{0.f, 0.f, 0.f, 0.f};
#pragma unroll
      for (int j = 0; j < 4; ++j) dd[ct] = mfma(asb8(ov[j]), wf[ct * 4 + j], dd[ct]);
    }
    float t4[4];
#pragma unroll
    for (int ct = 0; ct < 4; ++ct) { const f32x2 v = f32x2{dd[ct][0], dd[ct][1]} * f32x2{we[0], we[1]} + f32x2{dd[ct][2], dd[ct][3]} * f32x2{we[2], we[3]}; t4[ct] = v[0] + v[1]; }
    {
      auto a = __builtin_amdgcn_permlane32_swap(__float_as_uint(t4[0]), __float_as_uint(t4[1]), false, false), b = __builtin_amdgcn_permlane32_swap(__float_as_uint(t4[2]), __float_as_uint(t4[3]), false, false);
      const float u0 = __uint_as_float(a[0]) + __uint_as_float(a[1]), u1 = __uint_as_float(b[0]) + __uint_as_float(b[1]);
      auto c = __builtin_amdgcn_permlane16_swap(__float_as_uint(u0), __float_as_uint(u1), false, false);
      *(LAS float*)(L + la0) = __uint_as_float(c[0]) + __uint_as_float(c[1]);
    }
    CY(1); CY(2);
    wgbar();
    CY(3);
    if (wave == 0) BST64(pk4s((*(LAS f32x4*)(L + la1) + *(LAS f32x4*)(L + la1 + 256)) + (*(LAS f32x4*)(L + la1 + 512) + *(LAS f32x4*)(L + la1 + 768))), yo);
    CY(4); CYOUT(5);
    ST(8);
  } else if (roleB) {
    const u32x2 xw = *(const u32x2*)(x + wave * 128 + n16 * 8 + (gq & 1) * 4);
    f32x2 xa = unpk(xw[0]), xb = unpk(xw[1]);
    bf16x8 wf[24];
    lds_wait_ge(lc + C_WLAND, 1u);
    f32x4 cs = {0.f, 0.f, 0.f, 0.f};
    {
      const int ao = (8 * gq + q4) * 64 + ((wave ^ ((gq & 1) | (p4 & 2))) << 4) + (p4 & 1) * 8;
      LAS char* a0 = L + ((NTB + (p4 & 2)) & 7) * 16384 + ao; LAS char* a1 = L + ((NTB + (p4 & 2) + 1) & 7) * 16384 + ao;
#pragma unroll
      for (int j = 0; j < 8; ++j) { wf[j] = tr2(a0 + j * 2048, a0 + j * 2048 + 256); wf[8 + j] = tr2(a1 + j * 2048, a1 + j * 2048 + 256); }
      PIN8(wf); PIN8((wf + 8));
#pragma unroll
      for (int j = 0; j < 16; ++j) cs = mfma(wf[j], asb8(u32x4{0xbf80bf80u, 0xbf80bf80u, 0xbf80bf80u, 0xbf80bf80u}), cs);
    }
    LAS char* a2 = L + ((NTB + 4 + (gq >> 1)) & 7) * 16384 + (8 * (gq & 1) + q4) * 1024 + wave * 256 + p4 * 8;
    auto read_down = [&]() {
#pragma unroll
      for (int ct = 0; ct < 8; ++ct) { LAS char* a = a2 + ((ct ^ (q4 | ((gq & 1) << 2))) << 5); wf[16 + ct] = tr2(a, a + 4096); }
    };
#pragma unroll
    for (int ct = 0; ct < 8; ++ct) asm volatile("" : "=v"(wf[16 + ct]));
    bool down = false;
    unsigned aad[8];
#pragma unroll
    for (int ct = 0; ct < 8; ++ct) aad[ct] = (n16 & 7) == ct ? L_ACT + gq * 16 : L_ZERO;
    int zo = n16 < 8 ? WS_Z + bi * 2048 + (wave * 128 + n16 * 16 + gq * 4) * 4 : WS_END, ho = (bi == 0 && lane < 32) ? WS_Z + 56 * 2048 + (wave * 128 + n16 * 8 + gq * 4) * 4 : WS_END;
    unsigned lb0 = lane < 32 ? L_VEC + wave * 256 + n16 * 16 + gq * 8 : L_MRG + lane * 8, lb1 = L_VEC + lane * 16, lb2 = L_VEC + gq * 16, lb3 = (n16 == 0 && gq < 2) ? L_ACT + wave * 16 + gq * 8 : L_MRG + lane * 8;
    asm volatile("" : "+v"(xa), "+v"(xb), "+v"(zo), "+v"(ho), "+v"(lb0), "+v"(lb1), "+v"(lb2), "+v"(lb3));
    asm volatile("" : "+v"(cs), "+v"(aad[0]), "+v"(aad[1]), "+v"(aad[2]), "+v"(aad[3]), "+v"(aad[4]), "+v"(aad[5]), "+v"(aad[6]), "+v"(aad[7]));
    ST(6);
    u32x4 sel = {n16 == 0 ? 0x3f80u : n16 == 1 ? 0x3f800000u : 0u, n16 == 2 ? 0x3f80u : n16 == 3 ? 0x3f800000u : 0u, n16 == 4 ? 0x3f80u : n16 == 5 ? 0x3f800000u : 0u, n16 == 6 ? 0x3f80u : n16 == 7 ? 0x3f800000u : 0u};
    int yb = WS_Y + gq * 1024 + wave * 256 + n16 * 16;
    asm volatile("" : "+v"(sel), "+v"(yb));
    u32x4 yv[4];
#pragma unroll 1
    for (int sp = 0; sp < SPIN_G; ++sp) {
      asm volatile("" ::: "memory");
#pragma unroll
      for (int k = 0; k < 4; ++k) yv[k] = __builtin_amdgcn_raw_buffer_load_b128(rs, yb, k * 4096, 16);
      if (!down && lds_ld(lc + C_WLAND) >= 2u) { read_down(); down = true; }
      unsigned mx = 0;
#pragma unroll
      for (int k = 0; k < 4; ++k) mx = umax3(umax3(mx, yv[k][0], yv[k][1]), yv[k][2], yv[k][3]);
      if (__all(mx != SENT)) break;
    }
    ST(7);
    CY(0);
    f32x4 ys = {0.f, 0.f, 0.f, 0.f};
#pragma unroll
    for (int k = 0; k < 4; ++k) ys = mfma(asb8(sel), asb8(yv[k]), ys);
    const u32x2 hp = {pk2(unpk(pk2(ys[0], ys[1])) + xa), pk2(unpk(pk2(ys[2], ys[3])) + xb)};
    *(LAS u32x2*)(L + lb0) = hp;
    CY(1);
    wgbar();
    CY(2);
    if (!down) read_down();
    const u32x4 h8 = *(LAS u32x4*)(L + lb1);
    bf16x8 b[16];
#pragma unroll
    for (int j = 0; j < 16; ++j) b[j] = asb8(*(LAS u32x4*)(L + lb2 + j * 64));
    BST128((u32x4{hp[0] << 16, hp[0] & 0xffff0000u, hp[1] << 16, hp[1] & 0xffff0000u}), ho);
    __builtin_amdgcn_sched_barrier(0);
    f32x4 y0 = {0.f, 0.f, 0.f, 0.f};
#pragma unroll
    for (int j = 0; j < 16; ++j) y0 = mfma(wf[j], b[j], y0);
    float sh = 0.f, sq = 0.f;
#pragma unroll
    for (int k = 0; k < 4; ++k) { sh = dot2(h8[k], 0x3f803f80u, sh); sq = dot2(h8[k], h8[k], sq); }
    const float mean = wave_sum(sh) * (1.f / 512.f);
    const float var = fmx(fmaf(-mean, mean, wave_sum(sq) * (1.f / 512.f)), 0.f);
    const float mb = rbf(mean), rsd = __builtin_amdgcn_rcpf(rbf(__builtin_amdgcn_sqrtf(rbf(rbf(var) + 1e-5f))));
    const f32x4 gu = (y0 + cs * mb) * rsd;
    CY(3);
    const unsigned g0 = pk2(gu[0], gu[1]), g1 = pk2(gu[2], gu[3]);
    auto s0 = __builtin_amdgcn_permlane32_swap(g0, g0, false, false), s1 = __builtin_amdgcn_permlane32_swap(g1, g1, false, false);
    const f32x2 ga = unpk(s0[0]), gb = unpk(s1[0]);
    const unsigned a0 = pk2(f32x2{fmx(ga[0], 0.f), fmx(ga[1], 0.f)} * unpk(s0[1])), a1 = pk2(f32x2{fmx(gb[0], 0.f), fmx(gb[1], 0.f)} * unpk(s1[1]));
    *(LAS u32x2*)(L + lb3) = u32x2{a0, a1};
    CY(4);
    wgbar();
    bf16x8 ab[8];
#pragma unroll
    for (int ct = 0; ct < 8; ++ct) ab[ct] = asb8(*(LAS u32x4*)(L + aad[ct]));
    __builtin_amdgcn_sched_barrier(0);
    f32x4 z = {0.f, 0.f, 0.f, 0.f};
#pragma unroll
    for (int ct = 0; ct < 8; ++ct) z = mfma(wf[16 + ct], ab[ct], z);
    BST128(fns4(z), zo);
    CY(5); CYOUT(6);
    ST(8);
  } else if (roleC && wave == 0) {
    lds_wait_ge(lc + C_WLAND, 2u);
    const int zb = WS_Z + (lane >> 3) * 2048 + ci * 64 + (lane & 7) * 8;
    u32x2 zv[8];
#pragma unroll 1
    for (int sp = 0; sp < SPIN_G; ++sp) {
      asm volatile("" ::: "memory");
      zv[7] = BLD64(WS_Z + 56 * 2048 + ci * 64 + (lane & 7) * 8);
#pragma unroll
      for (int i = 0; i < 7; ++i) zv[i] = BLD64(zb + i * 16384);
      unsigned mx = 0;
#pragma unroll
      for (int i = 0; i < 8; ++i) mx = umax3(mx, zv[i][0], zv[i][1]);
      if (__all(mx != SENT)) break;
    }
    ST(7);
    const f32x2 t = ((asf2(zv[0]) + asf2(zv[1])) + (asf2(zv[2]) + asf2(zv[3]))) + ((asf2(zv[4]) + asf2(zv[5])) + asf2(zv[6]));
    float t0 = t[0], t1 = t[1];
    t0 += dpp<ROR8>(t0); t1 += dpp<ROR8>(t1);
    t0 = x32_sum(x16_sum(t0)); t1 = x32_sum(x16_sum(t1));
    const unsigned o = pk2(unpk(pk2(t0, t1)) + asf2(zv[7]));
    if (lane < 8) *(unsigned*)(out + ci * 16 + lane * 2) = o;
    if (ci == 0 && lane == 0) BST64(*(LAS u32x2*)(L + L_CTRL + C_EPOCH), WS_E);
    ST(8);
  }
  __builtin_amdgcn_s_waitcnt(0);
}

extern "C" void kernel_launch(void* const* d_in, const int* in_sizes, int n_in, void* d_out, int out_size, void* d_ws, size_t ws_size, hipStream_t stream) {
  fused_decode<<<NWG, NTHR, 0, stream>>>((const bf16_t*)d_in[0], (const bf16_t*)d_in[1], (const bf16_t*)d_in[2], (const bf16_t*)d_in[3], (const bf16_t*)d_in[4],
                                         (const bf16_t*)d_in[6], (const bf16_t*)d_in[7], (const bf16_t*)d_in[8], (bf16_t*)d_out, (char*)d_ws);
}
```

```cpp
#include <hip/hip_runtime.h>
#include <cstdint>

typedef unsigned short bf16_t;
typedef __attribute__((ext_vector_type(2))) __bf16 bf16x2;
typedef __attribute__((ext_vector_type(4))) __bf16 bf16x4;
typedef __attribute__((ext_vector_type(8))) __bf16 bf16x8;
typedef __attribute__((ext_vector_type(2))) unsigned u32x2;
typedef __attribute__((ext_vector_type(4))) unsigned u32x4;
typedef __attribute__((ext_vector_type(2))) float f32x2;
typedef __attribute__((ext_vector_type(4))) float f32x4;
typedef unsigned long long u64;
#define GAS __attribute__((address_space(1)))
#define LAS __attribute__((address_space(3)))

#define WIN 24
#define WMAX 48
#define WSH 12
#define TAU 540
#define PLEAD -2
#define QDELAY 600
#define WWIN 56
#define WEARLY 0
#define LIMA 32
#define LIMB 2
#define LIMH 0
#define ADELAY 12
#define SYNCT 1
#define LSEAT 20
#define SBITS 0x38C000
#define ALIGNV 1
#define AEARLY 1

constexpr int SK = 65536, F = 1792;
constexpr int NWG = 256, NTHR = 384;
constexpr int EB = 3, NTB = 64 - EB, NTA = 65;
constexpr int PACE = TAU * 256 / 20;
constexpr unsigned SMASK = SBITS, TMASK = 0xFFE000u & ~SMASK;
template <unsigned MASK> __device__ __forceinline__ unsigned deposit(unsigned v) {
  unsigned r = 0; int k = 0;
#pragma unroll
  for (int b = 13; b < 24; ++b) if ((MASK >> b) & 1) { r |= ((v >> k) & 1u) << b; ++k; }
  return r;
}
constexpr unsigned SENT = 0xFFFFFFFFu;
constexpr int WS_E = 0, WS_GF = 1024, WS_QF = 4096, WS_QP = 65536, WS_P1 = 131072, P1_REC = 1152, WS_Y = WS_P1 + NWG * P1_REC, WS_Z = WS_Y + 16384, WS_END = WS_Z + 57 * 2048;
constexpr int WS_TS = 1 << 20;
constexpr int L_CTRL = 131072, L_Q = L_CTRL + 256, L_QP = L_Q + 1024, L_XN = L_QP + 1024, L_SC = L_XN + 2048, L_ML = L_SC + 65 * 256 + 768, L_WE = L_ML + 128, L_RED = L_WE + 512,
              L_VEC = L_RED + 1024, L_ACT = L_VEC + 1024, L_ZERO = L_ACT + 64, L_JUNK = L_XN, L_TS = L_ZERO + 256, L_END = L_TS + 1536;
constexpr int C_CONS = 0, C_LANDED = 16, C_QSYNC = 20, C_QRDY = 24, C_MSYNC = 28, C_STEP = 32, C_WLAND = 36, C_SHIFT = 40, C_EPOCH = 128;
constexpr float LOG2E = 1.4426950408889634f;
constexpr int SPIN_G = 400000, SPIN_L = 8000000;

__device__ __forceinline__ float fmx(float a, float b) { return __builtin_elementwise_maximum(a, b); }
__device__ __forceinline__ float bflo(unsigned u) { return __builtin_bit_cast(float, u << 16); }
__device__ __forceinline__ float bfhi(unsigned u) { return __builtin_bit_cast(float, u & 0xffff0000u); }
__device__ __forceinline__ f32x2 unpk(unsigned u) { return f32x2{bflo(u), bfhi(u)}; }
__device__ __forceinline__ unsigned pk2(f32x2 v) { return __builtin_bit_cast(unsigned, __builtin_convertvector(v, bf16x2)); }
__device__ __forceinline__ unsigned pk2(float lo, float hi) { return pk2(f32x2{lo, hi}); }
__device__ __forceinline__ float rbf(float f) { return (float)(__bf16)f; }
__device__ __forceinline__ unsigned uns(unsigned u) { return min(u, 0xFFFFFFFEu); }
__device__ __forceinline__ unsigned fns(float f) { return uns(__builtin_bit_cast(unsigned, f)); }
__device__ __forceinline__ u32x4 fns4(f32x4 v) { return u32x4{fns(v[0]), fns(v[1]), fns(v[2]), fns(v[3])}; }
__device__ __forceinline__ u32x2 pk4s(f32x4 v) { return u32x2{uns(pk2(v[0], v[1])), uns(pk2(v[2], v[3]))}; }
__device__ __forceinline__ unsigned umax3(unsigned a, unsigned b, unsigned c) { return max(max(a, b), c); }
template <int CTRL> __device__ __forceinline__ float dpp(float x) { return __builtin_bit_cast(float, __builtin_amdgcn_mov_dpp(__builtin_bit_cast(int, x), CTRL, 0xf, 0xf, true)); }
constexpr int XOR1 = 0xB1, XOR2 = 0x4E, HMIR = 0x141, ROR8 = 0x128;
__device__ __forceinline__ float x16_max(float x) { auto s = __builtin_amdgcn_permlane16_swap(__float_as_uint(x), __float_as_uint(x), false, false); return fmx(__uint_as_float(s[0]), __uint_as_float(s[1])); }
__device__ __forceinline__ float x32_max(float x) { auto s = __builtin_amdgcn_permlane32_swap(__float_as_uint(x), __float_as_uint(x), false, false); return fmx(__uint_as_float(s[0]), __uint_as_float(s[1])); }
__device__ __forceinline__ float x16_sum(float x) { auto s = __builtin_amdgcn_permlane16_swap(__float_as_uint(x), __float_as_uint(x), false, false); return __uint_as_float(s[0]) + __uint_as_float(s[1]); }
__device__ __forceinline__ float x32_sum(float x) { auto s = __builtin_amdgcn_permlane32_swap(__float_as_uint(x), __float_as_uint(x), false, false); return __uint_as_float(s[0]) + __uint_as_float(s[1]); }
__device__ __forceinline__ float row_sum(float x) { x += dpp<XOR1>(x); x += dpp<XOR2>(x); x += dpp<HMIR>(x); x += dpp<ROR8>(x); return x; }
__device__ __forceinline__ float row_max(float x) { x = fmx(x, dpp<XOR1>(x)); x = fmx(x, dpp<XOR2>(x)); x = fmx(x, dpp<HMIR>(x)); x = fmx(x, dpp<ROR8>(x)); return x; }
__device__ __forceinline__ float wave_sum(float x) { return x32_sum(x16_sum(row_sum(x))); }
__device__ __forceinline__ float dot2(unsigned a, unsigned b, float c) { return __builtin_amdgcn_fdot2_f32_bf16(__builtin_bit_cast(bf16x2, a), __builtin_bit_cast(bf16x2, b), c, false); }
template <int N> __device__ __forceinline__ void waitvm() { asm volatile("s_waitcnt vmcnt(%0)" :: "i"(N) : "memory"); }
__device__ __forceinline__ void waitlgkm() { asm volatile("s_waitcnt lgkmcnt(0)" ::: "memory"); }
__device__ __forceinline__ void wgbar() { asm volatile("s_waitcnt lgkmcnt(0)\n\ts_barrier" ::: "memory"); }
__device__ __forceinline__ void lds_st(unsigned addr, unsigned v) { asm volatile("ds_write_b32 %0, %1" :: "v"(addr), "v"(v) : "memory"); }
__device__ __forceinline__ unsigned lds_ld(unsigned addr) { unsigned v; asm volatile("ds_read_b32 %0, %1\n\ts_waitcnt lgkmcnt(0)" : "=&v"(v) : "v"(addr) : "memory"); return __builtin_amdgcn_readfirstlane(v); }
__device__ __forceinline__ void lds_wait_ge(unsigned addr, unsigned want) {
#pragma unroll 1
  for (int sp = 0; sp < SPIN_L; ++sp) { if (lds_ld(addr) >= want) break; __builtin_amdgcn_s_sleep(1); } }
__device__ __forceinline__ void lds_spin_ge(unsigned addr, unsigned want) {
#pragma unroll 1
  for (int sp = 0; sp < 2 * SPIN_L; ++sp) { if (lds_ld(addr) >= want) break; } }
__device__ __forceinline__ int vm_count() { unsigned ib; asm volatile("s_getreg_b32 %0, hwreg(HW_REG_IB_STS)" : "=s"(ib) :: "memory"); return (ib & 15) | ((ib >> 18) & 48); }
__device__ __forceinline__ bf16x8 asb8(u32x4 v) { return __builtin_bit_cast(bf16x8, v); }
__device__ __forceinline__ f32x4 asf4(u32x4 v) { return __builtin_bit_cast(f32x4, v); }
__device__ __forceinline__ f32x2 asf2(u32x2 v) { return __builtin_bit_cast(f32x2, v); }
__device__ __forceinline__ f32x4 mfma(bf16x8 a, bf16x8 b, f32x4 c) { return __builtin_amdgcn_mfma_f32_16x16x32_bf16(a, b, c, 0, 0, 0); }
__device__ __forceinline__ bf16x8 tr2(LAS char* p0, LAS char* p1) {
  bf16x4 a = __builtin_amdgcn_ds_read_tr16_b64_v4bf16((LAS bf16x4*)p0), b = __builtin_amdgcn_ds_read_tr16_b64_v4bf16((LAS bf16x4*)p1);
  return __builtin_shufflevector(a, b, 0, 1, 2, 3, 4, 5, 6, 7);
}
#define GLDS(src, dst, off) __builtin_amdgcn_global_load_lds((const GAS unsigned*)(src), (LAS unsigned*)(dst), 16, (off), 2)
#define PIN8(a) asm volatile("s_waitcnt lgkmcnt(0)" : "+v"(a[0]), "+v"(a[1]), "+v"(a[2]), "+v"(a[3]), "+v"(a[4]), "+v"(a[5]), "+v"(a[6]), "+v"(a[7]) :: "memory")
#define BLD32(off) __builtin_amdgcn_raw_buffer_load_b32(rs, (off), 0, 16)
#define BLD64(off) __builtin_amdgcn_raw_buffer_load_b64(rs, (off), 0, 16)
#define BLD128(off) __builtin_amdgcn_raw_buffer_load_b128(rs, (off), 0, 16)
#define BST32(v, off) __builtin_amdgcn_raw_buffer_store_b32((v), rs, (off), 0, 16)
#define BST64(v, off) __builtin_amdgcn_raw_buffer_store_b64((v), rs, (off), 0, 16)
#define BST128(v, off) __builtin_amdgcn_raw_buffer_store_b128((v), rs, (off), 0, 16)
#define ST(i) do {} while (0)
#define CY(i) do {} while (0)
#define CYOUT(n) do {} while (0)

__device__ __forceinline__ u32x4 score_tile(const bf16x8 (&kf)[8], const bf16x8 (&qf)[4], float& m) {
  f32x4 s0 = {0.f, 0.f, 0.f, 0.f}, s1 = {0.f, 0.f, 0.f, 0.f};
#pragma unroll
  for (int j = 0; j < 4; ++j) { s0 = mfma(kf[2 * j], qf[j], s0); s1 = mfma(kf[2 * j + 1], qf[j], s1); }
  constexpr float SCALE = 0.08838834764831845f;
  const u32x4 sc = {pk2(unpk(pk2(s0[0], s0[1])) * SCALE), pk2(unpk(pk2(s0[2], s0[3])) * SCALE), pk2(unpk(pk2(s1[0], s1[1])) * SCALE), pk2(unpk(pk2(s1[2], s1[3])) * SCALE)};
#pragma unroll
  for (int i = 0; i < 4; ++i) m = fmx(fmx(m, bflo(sc[i])), bfhi(sc[i]));
  return sc;
}

__global__ void __launch_bounds__(NTHR, 1) fused_decode(const bf16_t* __restrict__ x, const bf16_t* __restrict__ kc, const bf16_t* __restrict__ vc, const bf16_t* __restrict__ wq,
                                                        const bf16_t* __restrict__ wo, const bf16_t* __restrict__ wg, const bf16_t* __restrict__ wu, const bf16_t* __restrict__ wd,
                                                        bf16_t* __restrict__ out, char* ws) {
  __shared__ __attribute__((aligned(1024))) char lds_raw[L_END];
  const u64 tk = __builtin_amdgcn_s_memrealtime();
  LAS char* L = (LAS char*)lds_raw;
  const unsigned lc = (unsigned)(size_t)L + L_CTRL;
  const int tid = threadIdx.x, lane = tid & 63, wave = __builtin_amdgcn_readfirstlane(tid >> 6), bid = blockIdx.x;
  const int g = bid & 7, s = bid >> 3, rec = g * 32 + s;
  const bool roleA = s < 16, roleB = s >= 16 && s < 23, roleC = s >= 28;
  const int bi = g * 7 + (s - 16), ci = g * 4 + (s - 28);
  const int NT = roleB ? NTB : roleC ? 64 : 65, NS = 2 * NT;
  const int xe = s < 16 ? s : s - 7;
  const unsigned sb = deposit<SMASK>(s), x0 = NT == 65 ? deposit<SMASK>(16 + xe / EB) | deposit<TMASK>(NTB + xe % EB) : sb | deposit<TMASK>(NT - 1);
  const unsigned off_e = (lane >> 4) * 256 + (((lane & 15) ^ ((lane >> 4) << 1)) << 4), off_o = off_e ^ 128;
  const char* ks = (const char*)kc + (size_t)g * (SK * 256);
  const char* vs = (const char*)vc + (size_t)g * (SK * 256);
  const __amdgpu_buffer_rsrc_t rs = __builtin_amdgcn_make_buffer_rsrc(ws, 0, WS_END, 0x00020000);
  if (wave == 4) {
    __builtin_amdgcn_s_setprio(3);
#pragma unroll
    for (int q = 0; q < 8; ++q) {
      const char* p = ks + ((q < 6 ? sb | deposit<TMASK>(q >> 1) : x0) + (q & 1) * 4096);
      GLDS(p + off_e, L + q * 4096, 0); GLDS(p + off_o, L + q * 4096, 1024); GLDS(p + off_e, L + q * 4096, 2048); GLDS(p + off_o, L + q * 4096, 3072);
    }
  } else if (wave < 4) {
    const u32x4 sv = {SENT, SENT, SENT, SENT};
    BST128(sv, tid < 72 ? WS_P1 + rec * P1_REC + tid * 16 : (roleA && tid >= 248) ? WS_Y + (g * 16 + s) * 128 + (tid - 248) * 16 : WS_END);
    if (roleB) BST128(sv, (tid >= 128 || bi == 0) ? WS_Z + (tid >= 128 ? bi : 56) * 2048 + (tid & 127) * 16 : WS_END);
    if (tid < 128) lds_st(tid < 64 ? lc + tid * 4 : lc - L_CTRL + L_ZERO + (tid - 64) * 4, 0u);
  }
  asm volatile("" :: "s"(x), "s"(wq), "s"(ws));
  wgbar();

  if (wave == 4) {
    const unsigned la = lc + C_LANDED;
    unsigned due = ((unsigned)tk << 8) + (4 - PLEAD) * PACE;
    auto step = [&](int i, const char* p, unsigned ca, int need) {
      unsigned a, b, c, v, w;
      asm volatile("1:\n\ts_memrealtime s[90:91]\n\tds_read_b32 %4, %12\n\ts_getreg_b32 %0, hwreg(HW_REG_IB_STS)\n\ts_bfe_u32 %1, %0, 0x20016\n\ts_and_b32 %0, %0, 15\n\ts_lshl4_add_u32 %0, %1, %0\n\t"
                   "s_sub_i32 %1, %6, %0\n\tv_mov_b32 %3, %1\n\tds_write_b32 %5, %3\n\ts_waitcnt lgkmcnt(0)\n\tv_cmp_gt_i32 vcc, %13, %4\n\ts_lshl_b32 %2, s90, 8\n\ts_sub_u32 %1, %7, %2\n\t"
                   "s_cmp_lt_u32 %1, %8\n\ts_cbranch_scc1 1b\n\t"
                   "s_sub_u32 %2, %2, %7\n\ts_lshr_b32 %1, %2, %9\n\ts_min_u32 %1, %1, %10\n\ts_add_u32 %1, %1, %11\n\t"
                   "s_cmp_gt_u32 %0, %1\n\ts_cbranch_scc1 1b\n\t"
                   "s_cbranch_vccz 2f\n\ts_lshr_b32 %2, %2, 30\n\ts_cbranch_scc0 1b\n2:"
                   : "=&s"(a), "=&s"(b), "=&s"(c), "=&v"(v), "=&v"(w) : "v"(la), "s"(8 * i), "s"(due), "s"(64 * PACE), "i"(WSH), "i"(WMAX - WIN), "i"(WIN - 8), "v"(ca), "s"(need)
                   : "memory", "scc", "vcc", "s90", "s91");
      due += PACE;
      LAS char* dst = L + (i & 15) * 8192;
      GLDS(p + off_e, dst, 0); GLDS(p + off_o, dst, 1024); GLDS(p + off_e, dst, 2048); GLDS(p + off_o, dst, 3072);
      GLDS(p + 4096 + off_e, dst + 4096, 0); GLDS(p + 4096 + off_o, dst + 4096, 1024); GLDS(p + 4096 + off_e, dst + 4096, 2048); GLDS(p + 4096 + off_o, dst + 4096, 3072);
    };
    int i = 4;
    unsigned to = deposit<TMASK>(3);
    const char* base = ks + sb;
#pragma unroll 1
    for (; i < NT + 16; ++i) {
      if (i == 40) due += lds_ld(lc + C_SHIFT);
      if (i == NT) { base = vs + sb; to = 0; if (ALIGNV == 2 || (ALIGNV == 1 && !roleB)) due += (65 - NT) * PACE; }
      const bool ex = i == NT + 3;
      step(i, ex ? vs + x0 : base + to, lc + C_CONS + (i & 3) * 4, i - 15);
      if (!ex) to = ((to | ~TMASK) + 1) & TMASK;
    }
    const unsigned call = lc + C_CONS + (lane & 3) * 4;
#pragma unroll 1
    for (; i < NS; ++i) {
      step(i, base + to, call, i - 15);
      to = ((to | ~TMASK) + 1) & TMASK;
      lds_st(lc + C_STEP, i);
    }
    waitvm<40>(); waitvm<32>(); lds_st(la, 8 * NS - 32);
    waitvm<24>(); lds_st(la, 8 * NS - 24);
    waitvm<16>(); lds_st(la, 8 * NS - 16);
    waitvm<8>(); lds_st(la, 8 * NS - 8);
    waitvm<0>(); lds_st(la, 8 * NS);
    waitlgkm();
    ST(2);
    return;
  }

  if (wave == 5) {
    const u32x2 ev = BLD64(WS_E);
    const u64 epoch = ((u64)(unsigned)__builtin_amdgcn_readfirstlane(ev[1]) << 32) | (unsigned)__builtin_amdgcn_readfirstlane(ev[0]);
    u64 z = epoch + 0x9E3779B97F4A7C15ull; z = (z ^ (z >> 30)) * 0xBF58476D1CE4E5B9ull; z = (z ^ (z >> 27)) * 0x94D049BB133111EBull; z ^= z >> 31;
    const unsigned thi = (unsigned)(z >> 32), tlo = (unsigned)z;
    if (lane == 0) *(LAS u32x2*)(L + L_CTRL + C_EPOCH) = u32x2{(unsigned)(epoch + 1), (unsigned)((epoch + 1) >> 32)};
    lds_wait_ge(lc + C_QSYNC, 4u);
    LAS float* qp = (LAS float*)(L + L_QP);
    BST32(__float_as_uint((qp[lane] + qp[64 + lane]) + (qp[128 + lane] + qp[192 + lane])), WS_QP + rec * 256 + lane * 4);
    waitvm<0>();
    if (lane < 2) BST64((lane ? u32x2{(unsigned)tk, tlo} : u32x2{tlo, thi}), WS_QF + rec * 128 + lane * 8);
    ST(2);
    while (__builtin_amdgcn_s_memrealtime() - tk < QDELAY) __builtin_amdgcn_s_sleep(4);
#pragma unroll 1
    for (int sp = 0; sp < SPIN_G; ++sp) {
      asm volatile("" ::: "memory");
      const u32x2 t = BLD64(lane < 32 ? WS_QF + (g * 32 + lane) * 128 : WS_QF + ((lane & 7) * 32 + s) * 128 + 8);
      if (((unsigned)__ballot((t[0] == tlo) & (t[1] == thi))) == 0xffffffffu) {
        int d = (lane >= 32 && t[1] == tlo) ? (int)(t[0] - (unsigned)tk) : 0;
        d = min(d, __builtin_amdgcn_mov_dpp(d, XOR1, 0xf, 0xf, true)); d = min(d, __builtin_amdgcn_mov_dpp(d, XOR2, 0xf, 0xf, true)); d = min(d, __builtin_amdgcn_mov_dpp(d, HMIR, 0xf, 0xf, true));
        lds_st(lc + C_SHIFT, max(__builtin_amdgcn_readlane(d, 32), -200) * 256);
        break;
      }
      __builtin_amdgcn_s_sleep(16);
    }
    asm volatile("" ::: "memory");
    {
      f32x4 a[4], b[4];
#pragma unroll
      for (int i = 0; i < 4; ++i) { const int o = WS_QP + (g * 32 + (lane >> 3) * 4 + i) * 256 + (lane & 7) * 32; a[i] = asf4(BLD128(o)); b[i] = asf4(BLD128(o + 16)); }
      const f32x4 sa = (a[0] + a[1]) + (a[2] + a[3]), sb = (b[0] + b[1]) + (b[2] + b[3]);
      *(LAS u32x4*)(L + L_Q + lane * 16) = u32x4{pk2(sa[0], sa[1]), pk2(sa[2], sa[3]), pk2(sb[0], sb[1]), pk2(sb[2], sb[3])};
    }
    waitlgkm();
    lds_st(lc + C_QRDY, 1u);
    ST(3);
    if (s == 23 && lane == 0) BST64((u32x2{tlo, thi}), WS_GF + g * 128);
    if (roleB || roleC) {
      while (__builtin_amdgcn_s_memrealtime() - tk < 2000) __builtin_amdgcn_s_sleep(32);
#pragma unroll 1
      for (int sp = 0; sp < SPIN_G; ++sp) {
        asm volatile("" ::: "memory");
        const u32x2 t = BLD64(WS_GF + (lane & 7) * 128);
        if (__all((t[0] == tlo) & (t[1] == thi))) break;
        __builtin_amdgcn_s_sleep(32);
      }
    }
    if (roleA) {
      lds_wait_ge(lc + C_LANDED, 8 * (NT + LSEAT));
      unsigned lse;
#pragma unroll 1
      for (int sp = 0; sp < SPIN_G; ++sp) {
        asm volatile("" ::: "memory");
        lse = BLD32(WS_P1 + (g * 32 + (lane & 31)) * P1_REC + 1024 + (2 * (s >> 3) + (lane >> 5)) * 4);
        if (__all(lse != SENT)) break;
        __builtin_amdgcn_s_sleep(32);
      }
      const float ls = __uint_as_float(lse);
      const float M = x16_max(row_max(ls));
      const float e = __builtin_amdgcn_exp2f(ls - M);
      *(LAS float*)(L + L_WE + lane * 4) = e * __builtin_amdgcn_rcpf(x16_sum(row_sum(e)));
    }
    __builtin_amdgcn_s_waitcnt(0);
    bool p1out = false;
    auto check_p1 = [&]() { unsigned v; asm volatile("ds_read_b32 %0, %1\n\ts_waitcnt lgkmcnt(0)" : "=&v"(v) : "v"(lc + C_CONS + (lane & 3) * 4) : "memory"); p1out = __all(v > (unsigned)NS); };
    auto hold = [&](int lim) {
#pragma unroll 1
      for (int sp = 0; sp < SPIN_L && !p1out && vm_count() > lim; ++sp) check_p1();
    };
    auto wunit = [&](int lim, int kw, const char* p, unsigned o0, unsigned o1, unsigned o2, unsigned o3, int stride4, unsigned x8) {
      const unsigned ca = lc + C_CONS + (lane & 3) * 4;
#pragma unroll 1
      for (int sp = 0; sp < SPIN_L; ++sp) {
        unsigned v; asm volatile("ds_read_b32 %0, %1\n\ts_waitcnt lgkmcnt(0)" : "=&v"(v) : "v"(ca) : "memory");
        if (__all(v >= (unsigned)(NS - 14 + 2 * kw))) break;
      }
      LAS char* dst = L + ((NT + kw) & 7) * 16384;
#pragma unroll
      for (int q = 0; q < 4; ++q) {
        hold(lim);
        waitvm<WWIN - 4>();
        if (q == 2) { o0 ^= x8; o1 ^= x8; o2 ^= x8; o3 ^= x8; }
        GLDS(p + o0, dst + q * 4096, 0); GLDS(p + o1, dst + q * 4096, 1024); GLDS(p + o2, dst + q * 4096, 2048); GLDS(p + o3, dst + q * 4096, 3072);
        p += stride4;
      }
    };
    constexpr int RS = F * 2;
    asm volatile("" :: "s"(wo), "s"(wg), "s"(wu), "s"(wd));
    if (roleA) {
      const unsigned lo = (lane >> 3) * 1024 + (((lane & 7) * 16) ^ (((lane >> 4) & 1) << 5));
      const char* p = (const char*)wo + (size_t)((4 * g + 2 * (s >> 3)) * 128) * 1024 + (s & 7) * 128;
      lds_wait_ge(lc + C_STEP, NS - 1 - AEARLY);
#pragma unroll 1
      for (int kw = 0; kw < 2; ++kw) wunit(LIMA, kw, p + kw * 131072, lo, (lo ^ 64) + 7168, lo + 2 * 7168, (lo ^ 64) + 3 * 7168, 32768, 0u);
    } else if (roleB) {
      constexpr int D1 = 16 * RS - 1024;
      lds_wait_ge(lc + C_STEP, NS - 1 - WEARLY);
#pragma unroll 1
      for (int kw = 0; kw < 4; ++kw) {
        const unsigned lo = (lane >> 2) * RS + (((lane & 3) ^ ((lane >> 5) | (kw & 2))) << 4);
        wunit(LIMB, kw, (const char*)((kw >> 1) ? wu : wg) + (size_t)((kw & 1) * 256) * RS + bi * 64, lo, lo + D1, lo + 2 * D1, lo + 3 * D1, 64 * RS, 0u);
      }
      const unsigned lo = lane * 16;
#pragma unroll 1
      for (int kw = 0; kw < 2; ++kw) wunit(LIMB, 4 + kw, (const char*)wd + (size_t)(bi * 32 + kw * 16) * 1024, lo, lo ^ 32, lo ^ 64, lo ^ 96, 4096, 128u);
      waitvm<32>(); lds_st(lc + C_WLAND, 1u);
    }
    waitvm<0>(); lds_st(lc + C_WLAND, 2u);
    ST(4);
    __builtin_amdgcn_s_waitcnt(0);
    return;
  }

  const int n16 = lane & 15, gq = lane >> 4, q4 = n16 >> 2, p4 = lane & 3;
  {
    u32x4 xv = *(const u32x4*)(x + lane * 8);
    u32x4 w[4];
    const int r0 = (s & 3) * 128 + (tid >> 3);
#pragma unroll
    for (int i = 0; i < 4; ++i) w[i] = __builtin_nontemporal_load((const u32x4*)(wq + (size_t)(r0 + 32 * i) * 4096 + g * 512 + (s >> 2) * 64 + (tid & 7) * 8));
    asm volatile("" : "+v"(xv) :: "memory");
    float sx = 0.f;
#pragma unroll
    for (int k = 0; k < 4; ++k) sx = dot2(xv[k], 0x3f803f80u, sx);
    const float mean = wave_sum(sx) * (1.f / 512.f);
    f32x2 d[4]; float sq = 0.f;
#pragma unroll
    for (int k = 0; k < 4; ++k) { d[k] = unpk(xv[k]); const f32x2 e = d[k] - mean; sq = fmaf(e[0], e[0], fmaf(e[1], e[1], sq)); }
    const float var = wave_sum(sq) * (1.f / 512.f);
    const float mb = rbf(mean), rsd = __builtin_amdgcn_rcpf(rbf(__builtin_amdgcn_sqrtf(rbf(rbf(var) + 1e-5f))));
#pragma unroll
    for (int k = 0; k < 4; ++k) d[k] = unpk(pk2(unpk(pk2(d[k] - mb)) * rsd));
    LAS float* xs = (LAS float*)(L + L_XN);
    *(LAS f32x4*)(xs + lane * 8) = f32x4{d[0][0], d[0][1], d[1][0], d[1][1]};
    *(LAS f32x4*)(xs + lane * 8 + 4) = f32x4{d[2][0], d[2][1], d[3][0], d[3][1]};
    float xr[4];
#pragma unroll
    for (int i = 0; i < 4; ++i) xr[i] = xs[r0 + 32 * i];
    asm volatile("" : "+v"(xr[0]), "+v"(xr[1]), "+v"(xr[2]), "+v"(xr[3]), "+v"(w[0]));
    f32x2 acc[4];
#pragma unroll
    for (int k = 0; k < 4; ++k) acc[k] = unpk(w[0][k]) * xr[0];
#pragma unroll
    for (int i = 1; i < 4; ++i)
#pragma unroll
      for (int k = 0; k < 4; ++k) acc[k] = unpk(w[i][k]) * xr[i] + acc[k];
    float a[8];
#pragma unroll
    for (int k = 0; k < 8; ++k) { float v = acc[k >> 1][k & 1]; v += dpp<ROR8>(v); a[k] = x32_sum(x16_sum(v)); }
    LAS float* qp = (LAS float*)(L + L_QP);
    if (lane < 8) { *(LAS f32x4*)(qp + wave * 64 + lane * 8) = f32x4{a[0], a[1], a[2], a[3]}; *(LAS f32x4*)(qp + wave * 64 + lane * 8 + 4) = f32x4{a[4], a[5], a[6], a[7]}; }
    asm volatile("s_waitcnt vmcnt(0) lgkmcnt(0)" ::: "memory");
    if (lane == 0) __hip_atomic_fetch_add((LAS unsigned*)(L + L_CTRL + C_QSYNC), 1u, __ATOMIC_RELAXED, __HIP_MEMORY_SCOPE_WORKGROUP);
    ST(1);
  }
  const unsigned cfree = lc + C_CONS + wave * 4;
  float inv;
  {
    unsigned koff[4];
#pragma unroll
    for (int j = 0; j < 4; ++j) koff[j] = n16 * 256 + (((gq ^ ((lane & 7) << 1)) ^ (4 * j)) << 4);
    auto read_k = [&](int t, bf16x8 (&kf)[8]) {
      lds_wait_ge(lc + C_LANDED, 8 * t + 8);
      LAS char* sl = L + (t & 15) * 8192;
#pragma unroll
      for (int j = 0; j < 4; ++j) { kf[2 * j] = asb8(*(LAS u32x4*)(sl + koff[j])); kf[2 * j + 1] = asb8(*(LAS u32x4*)(sl + koff[j] + 4096)); }
      PIN8(kf);
      lds_st(cfree, t + 1);
    };
    const unsigned so = n16 < 4 ? L_SC + gq * 64 + n16 * 16 : 0u;
    bf16x8 qf[4];
    float m = -3.0e38f;
    {
      bf16x8 k0[8], k1[8], k2[8], k3[8];
      read_k(wave, k0); read_k(wave + 4, k1); read_k(wave + 8, k2); read_k(wave + 12, k3);
      lds_wait_ge(lc + C_QRDY, 1u);
#pragma unroll
      for (int j = 0; j < 4; ++j) qf[j] = asb8(*(LAS u32x4*)(L + L_Q + (n16 & 3) * 256 + j * 64 + gq * 16));
      ST(3);
      const u32x4 c0 = score_tile(k0, qf, m), c1 = score_tile(k1, qf, m), c2 = score_tile(k2, qf, m), c3 = score_tile(k3, qf, m);
      if (n16 < 4) { LAS char* d = L + so + wave * 256; *(LAS u32x4*)d = c0; *(LAS u32x4*)(d + 1024) = c1; *(LAS u32x4*)(d + 2048) = c2; *(LAS u32x4*)(d + 3072) = c3; }
    }
#pragma unroll 1
    for (int t = wave + 16; t < NT; t += 4) {
      bf16x8 kf[8];
      read_k(t, kf);
      const u32x4 c = score_tile(kf, qf, m);
      if (n16 < 4) *(LAS u32x4*)(L + so + t * 256) = c;
    }
    m = x32_max(x16_max(m));
    if (lane < 4) *(LAS float*)(L + L_ML + lane * 16 + wave * 4) = m;
    waitlgkm();
    if (lane == 0) __hip_atomic_fetch_add((LAS unsigned*)(L + L_CTRL + C_MSYNC), 1u, __ATOMIC_RELAXED, __HIP_MEMORY_SCOPE_WORKGROUP);
    lds_wait_ge(lc + C_MSYNC, 4u);
    const f32x4 m4 = *(LAS f32x4*)(L + L_ML + p4 * 16);
    const float M = fmx(fmx(m4[0], m4[1]), fmx(m4[2], m4[3]));
    f32x2 ls = {0.f, 0.f};
#pragma unroll
    for (int i = 0; i < 5; ++i) {
      if (tid + 256 * i < 16 * NT) {
        u32x4 c = *(LAS u32x4*)(L + L_SC + tid * 16 + i * 4096);
#pragma unroll
        for (int k = 0; k < 4; ++k) { const f32x2 d = (unpk(c[k]) - M) * LOG2E; const f32x2 p = {__builtin_amdgcn_exp2f(d[0]), __builtin_amdgcn_exp2f(d[1])}; ls += p; c[k] = pk2(p); }
        *(LAS u32x4*)(L + L_SC + tid * 16 + i * 4096) = c;
      }
    }
    float l = ls[0] + ls[1];
    l += dpp<0x124>(l); l += dpp<ROR8>(l); l = x32_sum(x16_sum(l));
    if (lane < 4) *(LAS float*)(L + L_ML + 64 + lane * 16 + wave * 4) = l;
    waitlgkm();
    if (lane == 0) __hip_atomic_fetch_add((LAS unsigned*)(L + L_CTRL + C_MSYNC), 1u, __ATOMIC_RELAXED, __HIP_MEMORY_SCOPE_WORKGROUP);
    lds_wait_ge(lc + C_MSYNC, 8u);
    const f32x4 l4 = *(LAS f32x4*)(L + L_ML + 64 + p4 * 16);
    const float lt = (l4[0] + l4[1]) + (l4[2] + l4[3]);
    inv = __builtin_amdgcn_rcpf(lt);
    BST32(fns(fmaf(M, LOG2E, __builtin_amdgcn_logf(lt))), tid < 4 ? WS_P1 + rec * P1_REC + 1024 + lane * 4 : WS_END);
    ST(4);
  }
  {
    const unsigned va = (4 * gq + q4) * 256 + (((2 * wave) ^ (4 * (gq & 1) + q4)) << 5) + p4 * 8;
    int po = n16 < 8 ? WS_P1 + rec * P1_REC + wave * 256 + (n16 & 3) * 64 + (n16 & 4) * 8 + gq * 8 : WS_END;
    unsigned pa = (n16 & 4) ? L_ZERO : L_SC + gq * 64 + p4 * 16, pb = (n16 & 4) ? L_SC + gq * 64 + p4 * 16 : L_ZERO;
    const unsigned sta = (n16 & 4) ? 0u : 256u, stb = 256u - sta;
    asm volatile("" : "+v"(po), "+v"(inv));
    f32x4 o = {0.f, 0.f, 0.f, 0.f};
#pragma unroll 1
    for (int i = NT; i < NS; ++i) {
      bf16x8 p0 = *(LAS bf16x8*)(L + pa), p1 = *(LAS bf16x8*)(L + pb);
      pa += sta; pb += stb;
      unsigned a0 = (i & 15) * 8192 + va, a1 = a0 ^ 32;
      asm volatile("" : "+v"(a0), "+v"(a1));
      if (i < NS - 4) lds_wait_ge(lc + C_LANDED, 8 * i + 8); else lds_spin_ge(lc + C_LANDED, 8 * i + 8);
      bf16x8 v0 = tr2(L + a0, L + a0 + 4096), v1 = tr2(L + a1, L + a1 + 4096);
      asm volatile("s_waitcnt lgkmcnt(0)" : "+v"(v0), "+v"(v1), "+v"(p0), "+v"(p1) :: "memory");
      o = mfma(v1, p1, mfma(v0, p0, o));
      lds_st(cfree, i + 1);
    }
    BST64(pk4s(o * inv), po);
    lds_st(cfree, NS + 1);
  }
  ST(5);

  if (roleA) {
    const int hh = 2 * (s >> 3) + (wave >> 1), kt = wave & 1;
    const int pb = WS_P1 + (g * 32 + kt * 16 + n16) * P1_REC + hh * 64 + gq * 16;
    u32x4 ov[4]; bf16x8 wf[16]; f32x4 we;
#pragma unroll
    for (int i = 0; i < 16; ++i) asm volatile("" : "=v"(wf[i]));
    asm volatile("" : "=v"(ov[0]), "=v"(ov[1]), "=v"(ov[2]), "=v"(ov[3]), "=v"(we));
    int yo = tid < 16 ? WS_Y + (g * 16 + s) * 128 + tid * 8 : WS_END;
    unsigned la0 = L_RED + wave * 256 + ((gq >> 1) | ((gq & 1) << 1)) * 64 + n16 * 4, la1 = L_RED + n16 * 16;
    unsigned wa = ((NTA + (wave >> 1)) & 7) * 16384 + (8 * gq + q4) * 128 + p4 * 8, wx = ((q4 >> 1) | ((gq & 1) << 1)) << 5;
    asm volatile("" : "+v"(yo), "+v"(la0), "+v"(la1), "+v"(wa), "+v"(wx));
    if (ADELAY) __builtin_amdgcn_s_sleep(ADELAY);
    unsigned pend = 5u;
#pragma unroll 1
    for (int sp = 0; sp < SPIN_G; ++sp) {
      asm volatile("" ::: "memory");
      if (pend & 1u) {
#pragma unroll
        for (int j = 0; j < 4; ++j) ov[j] = BLD128(pb + j * 256);
      }
      if (pend & 4u) {
        bool w;
        do w = lds_ld(lc + C_WLAND) >= 2u; while (!w && vm_count() != 0);
        if (w) {
#pragma unroll
          for (int ct = 0; ct < 4; ++ct)
#pragma unroll
            for (int j = 0; j < 4; ++j) { LAS char* a = L + wa + ((ct << 5) ^ wx) + j * 4096; wf[ct * 4 + j] = tr2(a, a + 512); }
          we = *(LAS f32x4*)(L + L_WE + wave * 64 + gq * 16);
          pend &= ~4u;
          ST(6);
        }
      }
      if (pend & 1u) {
        unsigned mx = 0;
#pragma unroll
        for (int j = 0; j < 4; ++j) mx = umax3(umax3(mx, ov[j][0], ov[j][1]), ov[j][2], ov[j][3]);
        if (__all(mx != SENT)) pend &= ~1u;
      }
      if (!pend) break;
    }
    ST(7);
    CY(0);
    f32x4 dd[4];
#pragma unroll
    for (int ct = 0; ct < 4; ++ct) {
      dd[ct] = f32x4{0.f, 0.f, 0.f, 0.f};
#pragma unroll
      for (int j = 0; j < 4; ++j) dd[ct] = mfma(asb8(ov[j]), wf[ct * 4 + j], dd[ct]);
    }
    float t4[4];
#pragma unroll
    for (int ct = 0; ct < 4; ++ct) { const f32x2 v = f32x2{dd[ct][0], dd[ct][1]} * f32x2{we[0], we[1]} + f32x2{dd[ct][2], dd[ct][3]} * f32x2{we[2], we[3]}; t4[ct] = v[0] + v[1]; }
    {
      auto a = __builtin_amdgcn_permlane32_swap(__float_as_uint(t4[0]), __float_as_uint(t4[1]), false, false), b = __builtin_amdgcn_permlane32_swap(__float_as_uint(t4[2]), __float_as_uint(t4[3]), false, false);
      const float u0 = __uint_as_float(a[0]) + __uint_as_float(a[1]), u1 = __uint_as_float(b[0]) + __uint_as_float(b[1]);
      auto c = __builtin_amdgcn_permlane16_swap(__float_as_uint(u0), __float_as_uint(u1), false, false);
      *(LAS float*)(L + la0) = __uint_as_float(c[0]) + __uint_as_float(c[1]);
    }
    CY(1); CY(2);
    wgbar();
    CY(3);
    if (wave == 0) BST64(pk4s((*(LAS f32x4*)(L + la1) + *(LAS f32x4*)(L + la1 + 256)) + (*(LAS f32x4*)(L + la1 + 512) + *(LAS f32x4*)(L + la1 + 768))), yo);
    CY(4); CYOUT(5);
    ST(8);
  } else if (roleB) {
    const u32x2 xw = *(const u32x2*)(x + wave * 128 + n16 * 8 + (gq & 1) * 4);
    f32x2 xa = unpk(xw[0]), xb = unpk(xw[1]);
    bf16x8 wf[24];
    lds_wait_ge(lc + C_WLAND, 1u);
    f32x4 cs = {0.f, 0.f, 0.f, 0.f};
    {
      const int ao = (8 * gq + q4) * 64 + ((wave ^ ((gq & 1) | (p4 & 2))) << 4) + (p4 & 1) * 8;
      LAS char* a0 = L + ((NTB + (p4 & 2)) & 7) * 16384 + ao; LAS char* a1 = L + ((NTB + (p4 & 2) + 1) & 7) * 16384 + ao;
#pragma unroll
      for (int j = 0; j < 8; ++j) { wf[j] = tr2(a0 + j * 2048, a0 + j * 2048 + 256); wf[8 + j] = tr2(a1 + j * 2048, a1 + j * 2048 + 256); }
      PIN8(wf); PIN8((wf + 8));
#pragma unroll
      for (int j = 0; j < 16; ++j) cs = mfma(wf[j], asb8(u32x4{0xbf80bf80u, 0xbf80bf80u, 0xbf80bf80u, 0xbf80bf80u}), cs);
    }
    LAS char* a2 = L + ((NTB + 4 + (gq >> 1)) & 7) * 16384 + (8 * (gq & 1) + q4) * 1024 + wave * 256 + p4 * 8;
    auto read_down = [&]() {
#pragma unroll
      for (int ct = 0; ct < 8; ++ct) { LAS char* a = a2 + ((ct ^ (q4 | ((gq & 1) << 2))) << 5); wf[16 + ct] = tr2(a, a + 4096); }
    };
#pragma unroll
    for (int ct = 0; ct < 8; ++ct) asm volatile("" : "=v"(wf[16 + ct]));
    bool down = false;
    unsigned aad[8];
#pragma unroll
    for (int ct = 0; ct < 8; ++ct) aad[ct] = (n16 & 7) == ct ? L_ACT + gq * 16 : L_ZERO;
    int zo = n16 < 8 ? WS_Z + bi * 2048 + (wave * 128 + n16 * 16 + gq * 4) * 4 : WS_END, ho = (bi == 0 && lane < 32) ? WS_Z + 56 * 2048 + (wave * 128 + n16 * 8 + gq * 4) * 4 : WS_END;
    unsigned lb0 = lane < 32 ? L_VEC + wave * 256 + n16 * 16 + gq * 8 : L_JUNK + lane * 8, lb1 = L_VEC + lane * 16, lb2 = L_VEC + gq * 16, lb3 = (n16 == 0 && gq < 2) ? L_ACT + wave * 16 + gq * 8 : L_JUNK + lane * 8;
    asm volatile("" : "+v"(xa), "+v"(xb), "+v"(zo), "+v"(ho), "+v"(lb0), "+v"(lb1), "+v"(lb2), "+v"(lb3));
    asm volatile("" : "+v"(cs), "+v"(aad[0]), "+v"(aad[1]), "+v"(aad[2]), "+v"(aad[3]), "+v"(aad[4]), "+v"(aad[5]), "+v"(aad[6]), "+v"(aad[7]));
    ST(6);
    u32x4 sel = {n16 == 0 ? 0x3f80u : n16 == 1 ? 0x3f800000u : 0u, n16 == 2 ? 0x3f80u : n16 == 3 ? 0x3f800000u : 0u, n16 == 4 ? 0x3f80u : n16 == 5 ? 0x3f800000u : 0u, n16 == 6 ? 0x3f80u : n16 == 7 ? 0x3f800000u : 0u};
    int yb = WS_Y + gq * 1024 + wave * 256 + n16 * 16;
    asm volatile("" : "+v"(sel), "+v"(yb));
    u32x4 yv[4];
#pragma unroll 1
    for (int sp = 0; sp < SPIN_G; ++sp) {
      asm volatile("" ::: "memory");
#pragma unroll
      for (int k = 0; k < 4; ++k) yv[k] = __builtin_amdgcn_raw_buffer_load_b128(rs, yb, k * 4096, 16);
      if (!down && lds_ld(lc + C_WLAND) >= 2u) { read_down(); down = true; }
      unsigned mx = 0;
#pragma unroll
      for (int k = 0; k < 4; ++k) mx = umax3(umax3(mx, yv[k][0], yv[k][1]), yv[k][2], yv[k][3]);
      if (__all(mx != SENT)) break;
    }
    ST(7);
    CY(0);
    f32x4 ys = {0.f, 0.f, 0.f, 0.f};
#pragma unroll
    for (int k = 0; k < 4; ++k) ys = mfma(asb8(sel), asb8(yv[k]), ys);
    const u32x2 hp = {pk2(unpk(pk2(ys[0], ys[1])) + xa), pk2(unpk(pk2(ys[2], ys[3])) + xb)};
    *(LAS u32x2*)(L + lb0) = hp;
    CY(1);
    wgbar();
    CY(2);
    if (!down) read_down();
    const u32x4 h8 = *(LAS u32x4*)(L + lb1);
    bf16x8 b[16];
#pragma unroll
    for (int j = 0; j < 16; ++j) b[j] = asb8(*(LAS u32x4*)(L + lb2 + j * 64));
    BST128((u32x4{hp[0] << 16, hp[0] & 0xffff0000u, hp[1] << 16, hp[1] & 0xffff0000u}), ho);
    __builtin_amdgcn_sched_barrier(0);
    f32x4 y0 = {0.f, 0.f, 0.f, 0.f};
#pragma unroll
    for (int j = 0; j < 16; ++j) y0 = mfma(wf[j], b[j], y0);
    float sh = 0.f, sq = 0.f;
#pragma unroll
    for (int k = 0; k < 4; ++k) { sh = dot2(h8[k], 0x3f803f80u, sh); sq = dot2(h8[k], h8[k], sq); }
    const float mean = wave_sum(sh) * (1.f / 512.f);
    const float var = fmx(fmaf(-mean, mean, wave_sum(sq) * (1.f / 512.f)), 0.f);
    const float mb = rbf(mean), rsd = __builtin_amdgcn_rcpf(rbf(__builtin_amdgcn_sqrtf(rbf(rbf(var) + 1e-5f))));
    const f32x4 gu = (y0 + cs * mb) * rsd;
    CY(3);
    const unsigned g0 = pk2(gu[0], gu[1]), g1 = pk2(gu[2], gu[3]);
    auto s0 = __builtin_amdgcn_permlane32_swap(g0, g0, false, false), s1 = __builtin_amdgcn_permlane32_swap(g1, g1, false, false);
    const f32x2 ga = unpk(s0[0]), gb = unpk(s1[0]);
    const unsigned a0 = pk2(f32x2{fmx(ga[0], 0.f), fmx(ga[1], 0.f)} * unpk(s0[1])), a1 = pk2(f32x2{fmx(gb[0], 0.f), fmx(gb[1], 0.f)} * unpk(s1[1]));
    *(LAS u32x2*)(L + lb3) = u32x2{a0, a1};
    CY(4);
    wgbar();
    bf16x8 ab[8];
#pragma unroll
    for (int ct = 0; ct < 8; ++ct) ab[ct] = asb8(*(LAS u32x4*)(L + aad[ct]));
    __builtin_amdgcn_sched_barrier(0);
    f32x4 z = {0.f, 0.f, 0.f, 0.f};
#pragma unroll
    for (int ct = 0; ct < 8; ++ct) z = mfma(wf[16 + ct], ab[ct], z);
    BST128(fns4(z), zo);
    CY(5); CYOUT(6);
    ST(8);
  } else if (roleC && wave == 0) {
    lds_wait_ge(lc + C_WLAND, 2u);
    const int zb = WS_Z + (lane >> 3) * 2048 + ci * 64 + (lane & 7) * 8;
    u32x2 zv[8];
#pragma unroll 1
    for (int sp = 0; sp < SPIN_G; ++sp) {
      asm volatile("" ::: "memory");
      zv[7] = BLD64(WS_Z + 56 * 2048 + ci * 64 + (lane & 7) * 8);
#pragma unroll
      for (int i = 0; i < 7; ++i) zv[i] = BLD64(zb + i * 16384);
      unsigned mx = 0;
#pragma unroll
      for (int i = 0; i < 8; ++i) mx = umax3(mx, zv[i][0], zv[i][1]);
      if (__all(mx != SENT)) break;
    }
    ST(7);
    const f32x2 t = ((asf2(zv[0]) + asf2(zv[1])) + (asf2(zv[2]) + asf2(zv[3]))) + ((asf2(zv[4]) + asf2(zv[5])) + asf2(zv[6]));
    float t0 = t[0], t1 = t[1];
    t0 += dpp<ROR8>(t0); t1 += dpp<ROR8>(t1);
    t0 = x32_sum(x16_sum(t0)); t1 = x32_sum(x16_sum(t1));
    const unsigned o = pk2(unpk(pk2(t0, t1)) + asf2(zv[7]));
    if (lane < 8) *(unsigned*)(out + ci * 16 + lane * 2) = o;
    if (ci == 0 && lane == 0) BST64(*(LAS u32x2*)(L + L_CTRL + C_EPOCH), WS_E);
    ST(8);
  }
  __builtin_amdgcn_s_waitcnt(0);
}

extern "C" void kernel_launch(void* const* d_in, const int* in_sizes, int n_in, void* d_out, int out_size, void* d_ws, size_t ws_size, hipStream_t stream) {
  fused_decode<<<NWG, NTHR, 0, stream>>>((const bf16_t*)d_in[0], (const bf16_t*)d_in[1], (const bf16_t*)d_in[2], (const bf16_t*)d_in[3], (const bf16_t*)d_in[4],
                                         (const bf16_t*)d_in[6], (const bf16_t*)d_in[7], (const bf16_t*)d_in[8], (bf16_t*)d_out, (char*)d_ws);
}
```

```cpp
#include <hip/hip_runtime.h>
#include <cstdint>

typedef unsigned short bf16_t;
typedef __attribute__((ext_vector_type(2))) __bf16 bf16x2;
typedef __attribute__((ext_vector_type(4))) __bf16 bf16x4;
typedef __attribute__((ext_vector_type(8))) __bf16 bf16x8;
typedef __attribute__((ext_vector_type(2))) unsigned u32x2;
typedef __attribute__((ext_vector_type(4))) unsigned u32x4;
typedef __attribute__((ext_vector_type(2))) float f32x2;
typedef __attribute__((ext_vector_type(4))) float f32x4;
typedef unsigned long long u64;
#define GAS __attribute__((address_space(1)))
#define LAS __attribute__((address_space(3)))

#define WIN 24
#define WMAX 48
#define WSH 11
#define TAU 549
#define TAU0 600
#define ISW 36
#define T0 80
#define EARLYA 600
#define EARLYB 2400
#define NOAW 0
#define NOBW 0
#define BDELAY 0
#define ATIME 195
#define QSTAG 0
#define VTOUCH 0
#define OUTAUX -1
#define ENDWAIT 1
#define QSLEEP 16
#define QDELAY 520
#define WWIN 56
#define WEARLY 0
#define LSEAT 20
#define SBITS 0x38C000
#define AEARLY 0

constexpr int SK = 65536, F = 1792;
constexpr int NWG = 256, NTHR = 384;
constexpr int NT = 64, NS = 2 * NT, NTA = NT, NTB = NT;
constexpr int PACE0 = TAU0 * 256 / 20, PACE = TAU * 256 / 20, PACEA = PACE - EARLYA * 256 / 10 / (128 - ISW), PACEB = PACE - EARLYB * 256 / 10 / (128 - ISW);
constexpr unsigned SMASK = SBITS, TMASK = 0xFFE000u & ~SMASK;
template <unsigned MASK> __device__ __forceinline__ unsigned deposit(unsigned v) {
  unsigned r = 0; int k = 0;
#pragma unroll
  for (int b = 13; b < 24; ++b) if ((MASK >> b) & 1) { r |= ((v >> k) & 1u) << b; ++k; }
  return r;
}
constexpr unsigned SENT = 0xFFFFFFFFu;
constexpr int WS_E = 0, WS_GF = 1024, WS_TK = 2048, WS_QP = 4096, WS_P1 = WS_QP + NWG * 512, P1_REC = 1152, WS_Y = WS_P1 + NWG * P1_REC, WS_Z = WS_Y + 16384, WS_END = WS_Z + 57 * 2048;
constexpr int WS_TS = 1 << 20;
constexpr int L_CTRL = 131072, L_Q = L_CTRL + 256, L_QP = L_Q + 1024, L_XN = L_QP + 1024, L_SC = L_XN + 2048, L_TR = L_SC + 16384, L_ML = L_TR + 4608, L_WE = L_ML + 128, L_RED = L_WE + 512,
              L_VEC = L_RED + 1024, L_ACT = L_VEC + 1024, L_ZERO = L_ACT + 64, L_JUNK = L_XN, L_TS = L_ZERO + 256, L_END = L_TS + 1536;
constexpr int C_CONS = 0, C_LANDED = 16, C_QSYNC = 20, C_QRDY = 24, C_MSYNC = 28, C_STEP = 32, C_WLAND = 36, C_SHIFT = 40, C_TEND = 44, C_TAG = 128;
constexpr float LOG2E = 1.4426950408889634f;
constexpr int SPIN_G = 400000, SPIN_L = 8000000;

__device__ __forceinline__ float fmx(float a, float b) { return __builtin_elementwise_maximum(a, b); }
__device__ __forceinline__ float bflo(unsigned u) { return __builtin_bit_cast(float, u << 16); }
__device__ __forceinline__ float bfhi(unsigned u) { return __builtin_bit_cast(float, u & 0xffff0000u); }
__device__ __forceinline__ f32x2 unpk(unsigned u) { return f32x2{bflo(u), bfhi(u)}; }
__device__ __forceinline__ unsigned pk2(f32x2 v) { return __builtin_bit_cast(unsigned, __builtin_convertvector(v, bf16x2)); }
__device__ __forceinline__ unsigned pk2(float lo, float hi) { return pk2(f32x2{lo, hi}); }
__device__ __forceinline__ float rbf(float f) { return (float)(__bf16)f; }
__device__ __forceinline__ unsigned uns(unsigned u) { return min(u, 0xFFFFFFFEu); }
__device__ __forceinline__ unsigned fns(float f) { return uns(__builtin_bit_cast(unsigned, f)); }
__device__ __forceinline__ u32x4 fns4(f32x4 v) { return u32x4{fns(v[0]), fns(v[1]), fns(v[2]), fns(v[3])}; }
__device__ __forceinline__ u32x2 pk4s(f32x4 v) { return u32x2{uns(pk2(v[0], v[1])), uns(pk2(v[2], v[3]))}; }
__device__ __forceinline__ unsigned umax3(unsigned a, unsigned b, unsigned c) { return max(max(a, b), c); }
template <int CTRL> __device__ __forceinline__ float dpp(float x) { return __builtin_bit_cast(float, __builtin_amdgcn_mov_dpp(__builtin_bit_cast(int, x), CTRL, 0xf, 0xf, true)); }
constexpr int XOR1 = 0xB1, XOR2 = 0x4E, HMIR = 0x141, ROR8 = 0x128;
__device__ __forceinline__ float x16_max(float x) { auto s = __builtin_amdgcn_permlane16_swap(__float_as_uint(x), __float_as_uint(x), false, false); return fmx(__uint_as_float(s[0]), __uint_as_float(s[1])); }
__device__ __forceinline__ float x32_max(float x) { auto s = __builtin_amdgcn_permlane32_swap(__float_as_uint(x), __float_as_uint(x), false, false); return fmx(__uint_as_float(s[0]), __uint_as_float(s[1])); }
__device__ __forceinline__ float x16_sum(float x) { auto s = __builtin_amdgcn_permlane16_swap(__float_as_uint(x), __float_as_uint(x), false, false); return __uint_as_float(s[0]) + __uint_as_float(s[1]); }
__device__ __forceinline__ float x32_sum(float x) { auto s = __builtin_amdgcn_permlane32_swap(__float_as_uint(x), __float_as_uint(x), false, false); return __uint_as_float(s[0]) + __uint_as_float(s[1]); }
__device__ __forceinline__ float row_sum(float x) { x += dpp<XOR1>(x); x += dpp<XOR2>(x); x += dpp<HMIR>(x); x += dpp<ROR8>(x); return x; }
__device__ __forceinline__ float row_max(float x) { x = fmx(x, dpp<XOR1>(x)); x = fmx(x, dpp<XOR2>(x)); x = fmx(x, dpp<HMIR>(x)); x = fmx(x, dpp<ROR8>(x)); return x; }
__device__ __forceinline__ float wave_sum(float x) { return x32_sum(x16_sum(row_sum(x))); }
__device__ __forceinline__ float dot2(unsigned a, unsigned b, float c) { return __builtin_amdgcn_fdot2_f32_bf16(__builtin_bit_cast(bf16x2, a), __builtin_bit_cast(bf16x2, b), c, false); }
template <int N> __device__ __forceinline__ void waitvm() { asm volatile("s_waitcnt vmcnt(%0)" :: "i"(N) : "memory"); }
__device__ __forceinline__ void waitlgkm() { asm volatile("s_waitcnt lgkmcnt(0)" ::: "memory"); }
__device__ __forceinline__ void wgbar() { asm volatile("s_waitcnt lgkmcnt(0)\n\ts_barrier" ::: "memory"); }
__device__ __forceinline__ void lds_st(unsigned addr, unsigned v) { asm volatile("ds_write_b32 %0, %1" :: "v"(addr), "v"(v) : "memory"); }
__device__ __forceinline__ unsigned lds_ld(unsigned addr) { unsigned v; asm volatile("ds_read_b32 %0, %1\n\ts_waitcnt lgkmcnt(0)" : "=&v"(v) : "v"(addr) : "memory"); return __builtin_amdgcn_readfirstlane(v); }
__device__ __forceinline__ void lds_wait_ge(unsigned addr, unsigned want) {
#pragma unroll 1
  for (int sp = 0; sp < SPIN_L; ++sp) { if (lds_ld(addr) >= want) break; __builtin_amdgcn_s_sleep(1); } }
__device__ __forceinline__ void lds_spin_ge(unsigned addr, unsigned want) {
#pragma unroll 1
  for (int sp = 0; sp < 2 * SPIN_L; ++sp) { if (lds_ld(addr) >= want) break; } }
__device__ __forceinline__ int vm_count() { unsigned ib; asm volatile("s_getreg_b32 %0, hwreg(HW_REG_IB_STS)" : "=s"(ib) :: "memory"); return (ib & 15) | ((ib >> 18) & 48); }
__device__ __forceinline__ bf16x8 asb8(u32x4 v) { return __builtin_bit_cast(bf16x8, v); }
__device__ __forceinline__ f32x4 asf4(u32x4 v) { return __builtin_bit_cast(f32x4, v); }
__device__ __forceinline__ f32x2 asf2(u32x2 v) { return __builtin_bit_cast(f32x2, v); }
__device__ __forceinline__ f32x4 mfma(bf16x8 a, bf16x8 b, f32x4 c) { return __builtin_amdgcn_mfma_f32_16x16x32_bf16(a, b, c, 0, 0, 0); }
__device__ __forceinline__ bf16x8 tr2(LAS char* p0, LAS char* p1) {
  bf16x4 a = __builtin_amdgcn_ds_read_tr16_b64_v4bf16((LAS bf16x4*)p0), b = __builtin_amdgcn_ds_read_tr16_b64_v4bf16((LAS bf16x4*)p1);
  return __builtin_shufflevector(a, b, 0, 1, 2, 3, 4, 5, 6, 7);
}
#define GLDS(src, dst, off) __builtin_amdgcn_global_load_lds((const GAS unsigned*)(src), (LAS unsigned*)(dst), 16, (off), 2)
#define PIN8(a) asm volatile("s_waitcnt lgkmcnt(0)" : "+v"(a[0]), "+v"(a[1]), "+v"(a[2]), "+v"(a[3]), "+v"(a[4]), "+v"(a[5]), "+v"(a[6]), "+v"(a[7]) :: "memory")
#define BLD32(off) __builtin_amdgcn_raw_buffer_load_b32(rs, (off), 0, 16)
#define BLD64(off) __builtin_amdgcn_raw_buffer_load_b64(rs, (off), 0, 16)
#define BLD128(off) __builtin_amdgcn_raw_buffer_load_b128(rs, (off), 0, 16)
#define BST32(v, off) __builtin_amdgcn_raw_buffer_store_b32((v), rs, (off), 0, 16)
#define BST64(v, off) __builtin_amdgcn_raw_buffer_store_b64((v), rs, (off), 0, 16)
#define BST128(v, off) __builtin_amdgcn_raw_buffer_store_b128((v), rs, (off), 0, 16)
#define ST(i) do {} while (0)
#define TL(i) do {} while (0)
#define CY(i) do {} while (0)
#define CYOUT(n) do {} while (0)

__device__ __forceinline__ u32x4 score_tile(const bf16x8 (&kf)[8], const bf16x8 (&qf)[4], float& m) {
  f32x4 s0 = {0.f, 0.f, 0.f, 0.f}, s1 = {0.f, 0.f, 0.f, 0.f};
#pragma unroll
  for (int j = 0; j < 4; ++j) { s0 = mfma(kf[2 * j], qf[j], s0); s1 = mfma(kf[2 * j + 1], qf[j], s1); }
  constexpr float SCALE = 0.08838834764831845f;
  const u32x4 sc = {pk2(unpk(pk2(s0[0], s0[1])) * SCALE), pk2(unpk(pk2(s0[2], s0[3])) * SCALE), pk2(unpk(pk2(s1[0], s1[1])) * SCALE), pk2(unpk(pk2(s1[2], s1[3])) * SCALE)};
#pragma unroll
  for (int i = 0; i < 4; ++i) m = fmx(fmx(m, bflo(sc[i])), bfhi(sc[i]));
  return sc;
}

__global__ void __launch_bounds__(NTHR, 1) fused_decode(const bf16_t* __restrict__ x, const bf16_t* __restrict__ kc, const bf16_t* __restrict__ vc, const bf16_t* __restrict__ wq,
                                                        const bf16_t* __restrict__ wo, const bf16_t* __restrict__ wg, const bf16_t* __restrict__ wu, const bf16_t* __restrict__ wd,
                                                        bf16_t* __restrict__ out, char* ws) {
  __shared__ __attribute__((aligned(1024))) char lds_raw[L_END];
  LAS char* L = (LAS char*)lds_raw;
  const int lane = threadIdx.x & 63, hw = __builtin_amdgcn_readfirstlane(threadIdx.x >> 6), bid = blockIdx.x;
  const int g = bid & 7, s = bid >> 3;
  const unsigned sb = deposit<SMASK>(s);
  const unsigned off_e = (lane >> 4) * 256 + (((lane & 15) ^ ((lane >> 4) << 1)) << 4), off_o = off_e ^ 128;
  asm volatile("" :: "v"(off_e), "v"(off_o), "s"(hw), "s"(sb));
  __builtin_amdgcn_sched_barrier(0);
  const char* ks = (const char*)kc + (size_t)g * (SK * 256);
  asm volatile("" :: "s"(ks));
  const u64 tk = __builtin_amdgcn_s_memrealtime();
  if (hw == 0) {
#pragma unroll
    for (int q = 0; q < 6; ++q) {
      const char* p = ks + ((sb | deposit<TMASK>(q >> 1)) + (q & 1) * 4096);
      GLDS(p + off_e, L + q * 4096, 0); GLDS(p + off_o, L + q * 4096, 1024); GLDS(p + off_e, L + q * 4096, 2048); GLDS(p + off_o, L + q * 4096, 3072);
    }
    __builtin_amdgcn_s_setprio(3);
  }
  asm volatile("" ::: "memory");
  const int wave = hw == 0 ? 4 : hw == 5 ? 5 : hw - 1, tid = threadIdx.x - 64;
  const unsigned lc = (unsigned)(size_t)L + L_CTRL;
  const int rec = g * 32 + s;
  const bool roleA = g < 4, roleB = g >= 4 && s < 14, roleC = g < 4 && (s & 3) == 0;
  const int gq_ = g ^ 1, ga = s < 16 ? g ^ 1 : g + 4, sa = s & 15, bi = (g - 4) * 14 + s, ci = g * 8 + (s >> 2);
  const __amdgpu_buffer_rsrc_t rs = __builtin_amdgcn_make_buffer_rsrc(ws, 0, WS_END, 0x00020000);
  if (wave < 4) {
    const u32x4 sv = {SENT, SENT, SENT, SENT};
    BST128(sv, tid < 64 ? WS_P1 + rec * P1_REC + tid * 16 : (roleA && tid >= 248) ? WS_Y + (ga * 16 + sa) * 128 + (tid - 248) * 16 : WS_END);
    if (roleB) BST128(sv, (tid >= 128 || bi == 0) ? WS_Z + (tid >= 128 ? bi : 56) * 2048 + (tid & 127) * 16 : WS_END);
    if (tid < 128) lds_st(tid < 64 ? lc + tid * 4 : lc - L_CTRL + L_ZERO + (tid - 64) * 4, 0u);
  }
  asm volatile("" :: "s"(x), "s"(wq), "s"(ws));
  wgbar();

  if (wave == 4) {
    const unsigned la = lc + C_LANDED;
    const char* vs = (const char*)vc + (size_t)g * (SK * 256);
    unsigned due = ((unsigned)tk + T0) << 8, pace = PACE0;
    auto step = [&](int i, const char* p, unsigned ca, int need) {
      unsigned a, b, c, v, w;
      asm volatile("1:\n\ts_memrealtime s[90:91]\n\tds_read_b32 %4, %12\n\ts_getreg_b32 %0, hwreg(HW_REG_IB_STS)\n\ts_bfe_u32 %1, %0, 0x20016\n\ts_and_b32 %0, %0, 15\n\ts_lshl4_add_u32 %0, %1, %0\n\t"
                   "s_sub_i32 %1, %6, %0\n\tv_mov_b32 %3, %1\n\tds_write_b32 %5, %3\n\ts_waitcnt lgkmcnt(0)\n\tv_cmp_gt_i32 vcc, %13, %4\n\ts_lshl_b32 %2, s90, 8\n\ts_sub_u32 %1, %7, %2\n\t"
                   "s_cmp_lt_u32 %1, %8\n\ts_cbranch_scc1 1b\n\t"
                   "s_sub_u32 %2, %2, %7\n\ts_lshr_b32 %1, %2, %9\n\ts_min_u32 %1, %1, %10\n\ts_add_u32 %1, %1, %11\n\t"
                   "s_cmp_gt_u32 %0, %1\n\ts_cbranch_scc1 1b\n\t"
                   "s_cbranch_vccz 2f\n\ts_lshr_b32 %2, %2, 30\n\ts_cbranch_scc0 1b\n2:"
                   : "=&s"(a), "=&s"(b), "=&s"(c), "=&v"(v), "=&v"(w) : "v"(la), "s"(8 * i), "s"(due), "s"(64 * PACE), "i"(WSH), "i"(WMAX - WIN), "i"(WIN - 8), "v"(ca), "s"(need)
                   : "memory", "scc", "vcc", "s90", "s91");
      due += pace;
      LAS char* dst = L + (i & 15) * 8192;
      GLDS(p + off_e, dst, 0); GLDS(p + off_o, dst, 1024); GLDS(p + off_e, dst, 2048); GLDS(p + off_o, dst, 3072);
      GLDS(p + 4096 + off_e, dst + 4096, 0); GLDS(p + 4096 + off_o, dst + 4096, 1024); GLDS(p + 4096 + off_e, dst + 4096, 2048); GLDS(p + 4096 + off_o, dst + 4096, 3072);
    };
    int i = 3;
    unsigned to = deposit<TMASK>(3);
    const char* base = ks + sb;
#pragma unroll 1
    for (; i < NT + 16; ++i) {
      if (i == ISW) { due += lds_ld(lc + C_SHIFT); pace = roleA ? PACEA : roleB ? PACEB : PACE; }
      if (i == NT) base = vs + sb;
      step(i, base + to, lc + C_CONS + (i & 3) * 4, i - 15);
      to = ((to | ~TMASK) + 1) & TMASK;
    }
    const unsigned call = lc + C_CONS + (lane & 3) * 4;
#pragma unroll 1
    for (; i < NS; ++i) {
      step(i, base + to, call, i - 15);
      to = ((to | ~TMASK) + 1) & TMASK;
      lds_st(lc + C_STEP, i);
    }
    lds_st(lc + C_TEND, due + ATIME * 256);
    waitvm<32>(); lds_st(la, 8 * NS - 32);
    waitvm<24>(); lds_st(la, 8 * NS - 24);
    waitvm<16>(); lds_st(la, 8 * NS - 16);
    waitvm<8>(); lds_st(la, 8 * NS - 8);
    waitvm<0>(); lds_st(la, 8 * NS);
    waitlgkm();
    ST(2);
    return;
  }

  if (wave == 5) {
    const u32x2 ev = BLD64(WS_E);
    const u64 epoch = ((u64)(unsigned)__builtin_amdgcn_readfirstlane(ev[1]) << 32) | (unsigned)__builtin_amdgcn_readfirstlane(ev[0]);
    u64 z = epoch + 0x9E3779B97F4A7C15ull; z = (z ^ (z >> 30)) * 0xBF58476D1CE4E5B9ull; z = (z ^ (z >> 27)) * 0x94D049BB133111EBull; z ^= z >> 31;
    const unsigned thi = (unsigned)(z >> 32), tlo = (unsigned)z;
    if (lane == 0) *(LAS u32x2*)(L + L_CTRL + C_TAG) = u32x2{tlo, thi};
    if (s == 0 && lane == 0) BST64((u32x2{(unsigned)tk, tlo}), WS_TK + g * 128);
    lds_wait_ge(lc + C_QSYNC, 4u);
    {
      LAS f32x2* qp = (LAS f32x2*)(L + L_QP) + (lane & 31);
      const f32x2 v = (qp[0] + qp[32]) + (qp[64] + qp[96]);
      BST128((u32x4{__float_as_uint(v[0]), tlo, __float_as_uint(v[1]), thi}), lane < 32 ? WS_QP + rec * 512 + lane * 16 : WS_END);
    }
    ST(2);
    while (__builtin_amdgcn_s_memrealtime() - tk < QDELAY + (s & 7) * QSTAG) __builtin_amdgcn_s_sleep(4);
    {
      u32x4 v[16]; u32x2 t;
#pragma unroll 1
      for (int sp = 0; sp < SPIN_G; ++sp) {
        asm volatile("" ::: "memory");
#pragma unroll
        for (int i = 0; i < 16; ++i) v[i] = __builtin_amdgcn_raw_buffer_load_b128(rs, lane * 16 + (i & 3) * 1024, WS_QP + gq_ * 16384 + (i >> 2) * 4096, 16);
        t = BLD64(WS_TK + (lane & 7) * 128);
        unsigned bad = 0;
#pragma unroll
        for (int i = 0; i < 16; ++i) bad |= (v[i][1] ^ tlo) | (v[i][3] ^ thi);
        if (__all(bad == 0)) break;
        __builtin_amdgcn_s_sleep(QSLEEP);
      }
#pragma unroll
      for (int c = 0; c < 8; ++c) {
        const float a = x32_sum(__uint_as_float(v[2 * c][0]) + __uint_as_float(v[2 * c + 1][0])), b = x32_sum(__uint_as_float(v[2 * c][2]) + __uint_as_float(v[2 * c + 1][2]));
        *(LAS unsigned*)(L + L_Q + c * 128 + (lane & 31) * 4) = pk2(a, b);
      }
      int d = t[1] == tlo ? (int)(t[0] - (unsigned)tk) : 0;
      d = min(d, __builtin_amdgcn_mov_dpp(d, XOR1, 0xf, 0xf, true)); d = min(d, __builtin_amdgcn_mov_dpp(d, XOR2, 0xf, 0xf, true)); d = min(d, __builtin_amdgcn_mov_dpp(d, HMIR, 0xf, 0xf, true));
      lds_st(lc + C_SHIFT, min(max(d, -200), 0) * 256);
    }
    waitlgkm();
    lds_st(lc + C_QRDY, 1u);
    ST(3);
    if (VTOUCH) { unsigned t = __builtin_nontemporal_load((const unsigned*)((const char*)(lane < 4 ? vc : kc) + (size_t)g * (SK * 256) + sb + ((lane & 3) << 22))); asm volatile("" :: "v"(t)); }
    if (s == 23 && lane == 0) BST128((u32x4{0u, tlo, 0u, thi}), WS_GF + gq_ * 128);
    __builtin_amdgcn_s_waitcnt(0);
    auto wunit = [&](int kw, const char* p, unsigned o0, unsigned o1, unsigned o2, unsigned o3, int stride4, unsigned x8) {
      const unsigned ca = lc + C_CONS + (lane & 3) * 4;
#pragma unroll 1
      for (int sp = 0; sp < SPIN_L; ++sp) {
        unsigned v; asm volatile("ds_read_b32 %0, %1\n\ts_waitcnt lgkmcnt(0)" : "=&v"(v) : "v"(ca) : "memory");
        if (__all(v >= (unsigned)(NS - 14 + 2 * kw))) break;
      }
      LAS char* dst = L + ((NT + kw) & 7) * 16384;
#pragma unroll
      for (int q = 0; q < 4; ++q) {
        waitvm<WWIN - 4>();
        if (q == 2) { o0 ^= x8; o1 ^= x8; o2 ^= x8; o3 ^= x8; }
        GLDS(p + o0, dst + q * 4096, 0); GLDS(p + o1, dst + q * 4096, 1024); GLDS(p + o2, dst + q * 4096, 2048); GLDS(p + o3, dst + q * 4096, 3072);
        p += stride4;
      }
    };
    auto idle_until = [&](int st) {
#pragma unroll 1
      for (int sp = 0; sp < SPIN_L; ++sp) { const int c = lds_ld(lc + C_STEP); if (c >= st) break; if (c < st - 4) __builtin_amdgcn_s_sleep(16); }
    };
    auto all_tagged = [&](u32x4& t, int co) {
#pragma unroll 1
      for (int sp = 0; sp < SPIN_G && !__all((t[1] == tlo) & (t[3] == thi)); ++sp)
        asm volatile("buffer_load_dwordx4 %0, %1, %2, 0 offen sc1\n\ts_waitcnt vmcnt(0)" : "=&v"(t) : "v"(co), "s"(rs) : "memory");
    };
    constexpr int RS = F * 2;
    asm volatile("" :: "s"(wo), "s"(wg), "s"(wu), "s"(wd));
    if (roleA) {
      const unsigned lo = (lane >> 3) * 1024 + (((lane & 7) * 16) ^ (((lane >> 4) & 1) << 5));
      const char* p = (const char*)wo + (size_t)((4 * ga + 2 * (sa >> 3)) * 128) * 1024 + (s & 7) * 128;
      const int co = lane < 32 ? WS_P1 + (ga * 32 + lane) * P1_REC + 1024 + (sa >> 3) * 16 : WS_GF + (lane & 7) * 128;
      asm volatile("" :: "v"(co), "v"(lo), "s"(p));
      idle_until(NS - 1 - AEARLY);
      u32x4 t; asm volatile("buffer_load_dwordx4 %0, %1, %2, 0 offen sc1" : "=&v"(t) : "v"(co), "s"(rs) : "memory");
#pragma unroll 1
      for (int kw = 0; kw < (NOAW ? 0 : 2); ++kw) wunit(kw, p + kw * 131072, lo, (lo ^ 64) + 7168, lo + 2 * 7168, (lo ^ 64) + 3 * 7168, 32768, 0u);
      asm volatile("s_waitcnt vmcnt(32)" : "+v"(t) :: "memory");
      all_tagged(t, co);
      auto sw = __builtin_amdgcn_permlane32_swap(t[0], t[2], false, false);
      const float ls = __uint_as_float(sw[0]);
      const float M = x16_max(row_max(ls));
      const float e = __builtin_amdgcn_exp2f(ls - M);
      lds_st(lc - L_CTRL + L_WE + lane * 4, __float_as_uint(e * __builtin_amdgcn_rcpf(x16_sum(row_sum(e)))));
      lds_st(lc + C_QRDY, 2u);
    } else if (roleB) {
      constexpr int D1 = 16 * RS - 1024;
      const int co = WS_GF + (lane & 7) * 128;
      idle_until(NS - 1 - WEARLY);
      u32x4 t; asm volatile("buffer_load_dwordx4 %0, %1, %2, 0 offen sc1" : "=&v"(t) : "v"(co), "s"(rs) : "memory");
#pragma unroll 1
      for (int kw = 0; kw < (NOBW ? 0 : 4); ++kw) {
        const unsigned lo = (lane >> 2) * RS + (((lane & 3) ^ ((lane >> 5) | (kw & 2))) << 4);
        wunit(kw, (const char*)((kw >> 1) ? wu : wg) + (size_t)((kw & 1) * 256) * RS + bi * 64, lo, lo + D1, lo + 2 * D1, lo + 3 * D1, 64 * RS, 0u);
      }
      const unsigned lo = lane * 16;
#pragma unroll 1
      for (int kw = 0; kw < (NOBW ? 0 : 2); ++kw) wunit(4 + kw, (const char*)wd + (size_t)(bi * 32 + kw * 16) * 1024, lo, lo ^ 32, lo ^ 64, lo ^ 96, 4096, 128u);
      asm volatile("s_waitcnt vmcnt(32)" : "+v"(t) :: "memory");
      lds_st(lc + C_WLAND, 1u);
      all_tagged(t, co);
    } else if (bid == NWG - 1) {
      idle_until(NS - 40);
      u32x4 t = {0u, ~tlo, 0u, 0u};
      all_tagged(t, WS_GF + (lane & 7) * 128);
      if (lane == 0) BST64((u32x2{(unsigned)(epoch + 1), (unsigned)((epoch + 1) >> 32)}), WS_E);
    }
    waitvm<0>(); lds_st(lc + C_WLAND, 2u);
    ST(4);
    __builtin_amdgcn_s_waitcnt(0);
    return;
  }

  const int n16 = lane & 15, gq = lane >> 4, q4 = n16 >> 2, p4 = lane & 3;
  {
    u32x4 xv = *(const u32x4*)(x + lane * 8);
    u32x4 w[4];
    const int r0 = (s & 3) * 128 + (tid >> 3);
#pragma unroll
    for (int i = 0; i < 4; ++i) w[i] = __builtin_nontemporal_load((const u32x4*)(wq + (size_t)(r0 + 32 * i) * 4096 + gq_ * 512 + (s >> 2) * 64 + (tid & 7) * 8));
    asm volatile("" : "+v"(xv) :: "memory");
    float sx = 0.f;
#pragma unroll
    for (int k = 0; k < 4; ++k) sx = dot2(xv[k], 0x3f803f80u, sx);
    const float mean = wave_sum(sx) * (1.f / 512.f);
    f32x2 d[4]; float sq = 0.f;
#pragma unroll
    for (int k = 0; k < 4; ++k) { d[k] = unpk(xv[k]); const f32x2 e = d[k] - mean; sq = fmaf(e[0], e[0], fmaf(e[1], e[1], sq)); }
    const float var = wave_sum(sq) * (1.f / 512.f);
    const float mb = rbf(mean), rsd = __builtin_amdgcn_rcpf(rbf(__builtin_amdgcn_sqrtf(rbf(rbf(var) + 1e-5f))));
#pragma unroll
    for (int k = 0; k < 4; ++k) d[k] = unpk(pk2(unpk(pk2(d[k] - mb)) * rsd));
    LAS float* xs = (LAS float*)(L + L_XN);
    *(LAS f32x4*)(xs + lane * 8) = f32x4{d[0][0], d[0][1], d[1][0], d[1][1]};
    *(LAS f32x4*)(xs + lane * 8 + 4) = f32x4{d[2][0], d[2][1], d[3][0], d[3][1]};
    float xr[4];
#pragma unroll
    for (int i = 0; i < 4; ++i) xr[i] = xs[r0 + 32 * i];
    asm volatile("" : "+v"(xr[0]), "+v"(xr[1]), "+v"(xr[2]), "+v"(xr[3]), "+v"(w[0]));
    f32x2 acc[4];
#pragma unroll
    for (int k = 0; k < 4; ++k) acc[k] = unpk(w[0][k]) * xr[0];
#pragma unroll
    for (int i = 1; i < 4; ++i)
#pragma unroll
      for (int k = 0; k < 4; ++k) acc[k] = unpk(w[i][k]) * xr[i] + acc[k];
    float a[8];
#pragma unroll
    for (int k = 0; k < 8; ++k) { float v = acc[k >> 1][k & 1]; v += dpp<ROR8>(v); a[k] = x32_sum(x16_sum(v)); }
    LAS float* qp = (LAS float*)(L + L_QP);
    if (lane < 8) { *(LAS f32x4*)(qp + wave * 64 + lane * 8) = f32x4{a[0], a[1], a[2], a[3]}; *(LAS f32x4*)(qp + wave * 64 + lane * 8 + 4) = f32x4{a[4], a[5], a[6], a[7]}; }
    asm volatile("s_waitcnt vmcnt(0) lgkmcnt(0)" ::: "memory");
    if (lane == 0) __hip_atomic_fetch_add((LAS unsigned*)(L + L_CTRL + C_QSYNC), 1u, __ATOMIC_RELAXED, __HIP_MEMORY_SCOPE_WORKGROUP);
    ST(1);
  }
  const unsigned cfree = lc + C_CONS + wave * 4;
  float inv;
  {
    unsigned koff[4];
#pragma unroll
    for (int j = 0; j < 4; ++j) koff[j] = n16 * 256 + (((gq ^ ((lane & 7) << 1)) ^ (4 * j)) << 4);
    auto read_k = [&](int t, bf16x8 (&kf)[8]) {
      lds_wait_ge(lc + C_LANDED, 8 * t + 8);
      TL(t);
      LAS char* sl = L + (t & 15) * 8192;
#pragma unroll
      for (int j = 0; j < 4; ++j) { kf[2 * j] = asb8(*(LAS u32x4*)(sl + koff[j])); kf[2 * j + 1] = asb8(*(LAS u32x4*)(sl + koff[j] + 4096)); }
      PIN8(kf);
      lds_st(cfree, t + 1);
    };
    const unsigned so = n16 < 4 ? L_SC + gq * 64 + n16 * 16 : 0u;
    bf16x8 qf[4];
    float m = -3.0e38f;
    {
      bf16x8 k0[8], k1[8], k2[8], k3[8];
      read_k(wave, k0); read_k(wave + 4, k1); read_k(wave + 8, k2); read_k(wave + 12, k3);
      lds_wait_ge(lc + C_QRDY, 1u);
#pragma unroll
      for (int j = 0; j < 4; ++j) qf[j] = asb8(*(LAS u32x4*)(L + L_Q + (n16 & 3) * 256 + j * 64 + gq * 16));
      ST(3);
      const u32x4 c0 = score_tile(k0, qf, m), c1 = score_tile(k1, qf, m), c2 = score_tile(k2, qf, m), c3 = score_tile(k3, qf, m);
      if (n16 < 4) { LAS char* d = L + so + wave * 256; *(LAS u32x4*)d = c0; *(LAS u32x4*)(d + 1024) = c1; *(LAS u32x4*)(d + 2048) = c2; *(LAS u32x4*)(d + 3072) = c3; }
    }
#pragma unroll 1
    for (int t = wave + 16; t < NT; t += 4) {
      bf16x8 kf[8];
      read_k(t, kf);
      const u32x4 c = score_tile(kf, qf, m);
      if (n16 < 4) *(LAS u32x4*)(L + so + t * 256) = c;
    }
    m = x32_max(x16_max(m));
    if (lane < 4) *(LAS float*)(L + L_ML + lane * 16 + wave * 4) = m;
    waitlgkm();
    if (lane == 0) __hip_atomic_fetch_add((LAS unsigned*)(L + L_CTRL + C_MSYNC), 1u, __ATOMIC_RELAXED, __HIP_MEMORY_SCOPE_WORKGROUP);
    lds_wait_ge(lc + C_MSYNC, 4u);
    const f32x4 m4 = *(LAS f32x4*)(L + L_ML + p4 * 16);
    const float M = fmx(fmx(m4[0], m4[1]), fmx(m4[2], m4[3]));
    f32x2 ls = {0.f, 0.f};
#pragma unroll 1
    for (int i = 0; i < 4; ++i) {
      {
        u32x4 c = *(LAS u32x4*)(L + L_SC + tid * 16 + i * 4096);
#pragma unroll
        for (int k = 0; k < 4; ++k) { const f32x2 d = (unpk(c[k]) - M) * LOG2E; const f32x2 p = {__builtin_amdgcn_exp2f(d[0]), __builtin_amdgcn_exp2f(d[1])}; ls += p; c[k] = pk2(p); }
        *(LAS u32x4*)(L + L_SC + tid * 16 + i * 4096) = c;
      }
    }
    float l = ls[0] + ls[1];
    l += dpp<0x124>(l); l += dpp<ROR8>(l); l = x32_sum(x16_sum(l));
    if (lane < 4) *(LAS float*)(L + L_ML + 64 + lane * 16 + wave * 4) = l;
    waitlgkm();
    if (lane == 0) __hip_atomic_fetch_add((LAS unsigned*)(L + L_CTRL + C_MSYNC), 1u, __ATOMIC_RELAXED, __HIP_MEMORY_SCOPE_WORKGROUP);
    lds_spin_ge(lc + C_MSYNC, 8u);
    const f32x4 l4 = *(LAS f32x4*)(L + L_ML + 64 + p4 * 16);
    const float lt = (l4[0] + l4[1]) + (l4[2] + l4[3]);
    inv = __builtin_amdgcn_rcpf(lt);
    BST64((u32x2{__float_as_uint(fmaf(M, LOG2E, __builtin_amdgcn_logf(lt))), *(LAS unsigned*)(L + L_CTRL + C_TAG + (lane & 1) * 4)}), tid < 4 ? WS_P1 + rec * P1_REC + 1024 + lane * 8 : WS_END);
    ST(4);
  }
  int pbA = WS_P1 + (ga * 32 + (wave & 1) * 16 + (lane >> 2)) * P1_REC + (2 * (sa >> 3) + (wave >> 1)) * 64 + p4 * 16;
  {
    const unsigned va = (4 * gq + q4) * 256 + (((2 * wave) ^ (4 * (gq & 1) + q4)) << 5) + p4 * 8;
    int po = n16 < 8 ? WS_P1 + rec * P1_REC + wave * 256 + (n16 & 3) * 64 + (n16 & 4) * 8 + gq * 8 : WS_END;
    unsigned pa = (n16 & 4) ? L_ZERO : L_SC + gq * 64 + p4 * 16, pb = (n16 & 4) ? L_SC + gq * 64 + p4 * 16 : L_ZERO;
    const unsigned sta = (n16 & 4) ? 0u : 256u, stb = 256u - sta;
    asm volatile("" : "+v"(po), "+v"(inv), "+v"(pbA));
    f32x4 o = {0.f, 0.f, 0.f, 0.f};
    int seen = 0;
#pragma unroll 1
    for (int i = NT; i < NS; ++i) {
      bf16x8 p0 = *(LAS bf16x8*)(L + pa), p1 = *(LAS bf16x8*)(L + pb);
      pa += sta; pb += stb;
      unsigned a0 = (i & 15) * 8192 + va, a1 = a0 ^ 32;
      asm volatile("" : "+v"(a0), "+v"(a1));
      if (seen < 8 * i + 8) {
#pragma unroll 1
        for (int sp = 0; sp < 2 * SPIN_L; ++sp) { seen = lds_ld(lc + C_LANDED); if (seen >= 8 * i + 8) break; if (i < NS - 4) __builtin_amdgcn_s_sleep(1); }
      }
      TL(i);
      bf16x8 v0 = tr2(L + a0, L + a0 + 4096), v1 = tr2(L + a1, L + a1 + 4096);
      asm volatile("s_waitcnt lgkmcnt(0)" : "+v"(v0), "+v"(v1), "+v"(p0), "+v"(p1) :: "memory");
      o = mfma(v1, p1, mfma(v0, p0, o));
      lds_st(cfree, i + 1);
    }
    BST64(pk4s(o * inv), po);
  }
  ST(5);

  if (roleA) {
    u32x4 ov[4]; bf16x8 wf[16]; f32x4 we;
    {
      const unsigned tend = lds_ld(lc + C_TEND);
      lds_spin_ge(lc + C_QRDY, 2u);
      while (tend - ((unsigned)__builtin_amdgcn_s_memrealtime() << 8) < (1u << 16)) {}
    }
#pragma unroll
    for (int j = 0; j < 4; ++j) ov[j] = BLD128(pbA + j * 256);
    ST(13);
    __builtin_amdgcn_sched_barrier(0);
#pragma unroll
    for (int i = 0; i < 16; ++i) asm volatile("" : "=v"(wf[i]));
    u32x4 of[4];
    asm volatile("" : "=v"(we), "=v"(of[0]), "=v"(of[1]), "=v"(of[2]), "=v"(of[3]));
    int yo = tid < 16 ? WS_Y + (ga * 16 + sa) * 128 + tid * 8 : WS_END;
    unsigned la0 = L_RED + wave * 256 + ((gq >> 1) | ((gq & 1) << 1)) * 64 + n16 * 4, la1 = L_RED + n16 * 16;
    unsigned wa = ((NTA + (wave >> 1)) & 7) * 16384 + (8 * gq + q4) * 128 + p4 * 8, wx = ((q4 >> 1) | ((gq & 1) << 1)) << 5;
    unsigned tw = L_TR + wave * 1152 + p4 * 288 + (lane >> 2) * 16, tr = L_TR + wave * 1152 + gq * 288 + n16 * 16;
    asm volatile("" : "+v"(yo), "+v"(la0), "+v"(la1), "+v"(wa), "+v"(wx), "+v"(tw), "+v"(tr));
    unsigned pend = 5u;
#pragma unroll 1
    for (int sp = 0; sp < SPIN_G; ++sp) {
      if (pend & 4u) {
        bool w;
        do w = lds_ld(lc + C_WLAND) >= 2u; while (!w && vm_count() != 0);
        if (w) {
#pragma unroll
          for (int ct = 0; ct < 4; ++ct)
#pragma unroll
            for (int j = 0; j < 4; ++j) { LAS char* a = L + wa + ((ct << 5) ^ wx) + j * 4096; wf[ct * 4 + j] = tr2(a, a + 512); }
          we = *(LAS f32x4*)(L + L_WE + wave * 64 + gq * 16);
          pend &= ~4u;
          ST(6);
        }
      }
      if (pend & 1u) {
        unsigned mx = 0;
#pragma unroll
        for (int j = 0; j < 4; ++j) { *(LAS u32x4*)(L + tw) = ov[j]; of[j] = *(LAS u32x4*)(L + tr); mx = umax3(umax3(mx, ov[j][0], ov[j][1]), ov[j][2], ov[j][3]); }
        if (__all(mx != SENT)) pend &= ~1u;
      }
      if (!pend) {
        break; }
      asm volatile("" ::: "memory");
      if (pend & 1u) {
#pragma unroll
        for (int j = 0; j < 4; ++j) ov[j] = BLD128(pbA + j * 256);
      }
    }
    ST(7);
    CY(0);
    f32x4 dd[4];
#pragma unroll
    for (int ct = 0; ct < 4; ++ct) {
      dd[ct] = f32x4{0.f, 0.f, 0.f, 0.f};
#pragma unroll
      for (int j = 0; j < 4; ++j) dd[ct] = mfma(asb8(of[j]), wf[ct * 4 + j], dd[ct]);
    }
    float t4[4];
#pragma unroll
    for (int ct = 0; ct < 4; ++ct) { const f32x2 v = f32x2{dd[ct][0], dd[ct][1]} * f32x2{we[0], we[1]} + f32x2{dd[ct][2], dd[ct][3]} * f32x2{we[2], we[3]}; t4[ct] = v[0] + v[1]; }
    {
      auto a = __builtin_amdgcn_permlane32_swap(__float_as_uint(t4[0]), __float_as_uint(t4[1]), false, false), b = __builtin_amdgcn_permlane32_swap(__float_as_uint(t4[2]), __float_as_uint(t4[3]), false, false);
      const float u0 = __uint_as_float(a[0]) + __uint_as_float(a[1]), u1 = __uint_as_float(b[0]) + __uint_as_float(b[1]);
      auto c = __builtin_amdgcn_permlane16_swap(__float_as_uint(u0), __float_as_uint(u1), false, false);
      *(LAS float*)(L + la0) = __uint_as_float(c[0]) + __uint_as_float(c[1]);
    }
    CY(1); CY(2);
    wgbar();
    CY(3);
    if (wave == 0) BST64(pk4s((*(LAS f32x4*)(L + la1) + *(LAS f32x4*)(L + la1 + 256)) + (*(LAS f32x4*)(L + la1 + 512) + *(LAS f32x4*)(L + la1 + 768))), yo);
    CY(4); CYOUT(5);
    ST(8);
  } else if (roleB) {
    const u32x2 xw = *(const u32x2*)(x + wave * 128 + n16 * 8 + (gq & 1) * 4);
    f32x2 xa = unpk(xw[0]), xb = unpk(xw[1]);
    bf16x8 wf[24];
    lds_wait_ge(lc + C_WLAND, 1u);
    f32x4 cs = {0.f, 0.f, 0.f, 0.f};
    {
      const int ao = (8 * gq + q4) * 64 + ((wave ^ ((gq & 1) | (p4 & 2))) << 4) + (p4 & 1) * 8;
      LAS char* a0 = L + ((NTB + (p4 & 2)) & 7) * 16384 + ao; LAS char* a1 = L + ((NTB + (p4 & 2) + 1) & 7) * 16384 + ao;
#pragma unroll
      for (int j = 0; j < 8; ++j) { wf[j] = tr2(a0 + j * 2048, a0 + j * 2048 + 256); wf[8 + j] = tr2(a1 + j * 2048, a1 + j * 2048 + 256); }
      PIN8(wf); PIN8((wf + 8));
#pragma unroll
      for (int j = 0; j < 16; ++j) cs = mfma(asb8(u32x4{0xbf80bf80u, 0xbf80bf80u, 0xbf80bf80u, 0xbf80bf80u}), wf[j], cs);
    }
    LAS char* a2 = L + ((NTB + 4 + (gq >> 1)) & 7) * 16384 + (8 * (gq & 1) + q4) * 1024 + wave * 256 + p4 * 8;
    auto read_down = [&]() {
#pragma unroll
      for (int ct = 0; ct < 8; ++ct) { LAS char* a = a2 + ((ct ^ (q4 | ((gq & 1) << 2))) << 5); wf[16 + ct] = tr2(a, a + 4096); }
    };
    unsigned aad[4];
#pragma unroll
    for (int q = 0; q < 4; ++q) aad[q] = q4 == q ? L_ACT + gq * 16 : L_ZERO;
    int zo = WS_Z + bi * 2048 + tid * 8,
        ho = (bi == 0 && lane < 32) ? WS_Z + 56 * 2048 + (wave * 128 + n16 * 8 + gq * 4) * 4 : WS_END;
    unsigned lb0 = lane < 32 ? L_VEC + wave * 256 + n16 * 16 + gq * 8 : L_JUNK + lane * 8, lb1 = L_VEC + lane * 16, lb2[4], lb3 = lane < 8 ? L_ACT + wave * 16 + lane * 2 : L_JUNK + lane * 2;
#pragma unroll
    for (int q = 0; q < 4; ++q) lb2[q] = q4 == q ? L_VEC + n16 * 64 + gq * 16 : L_ZERO;
    asm volatile("" : "+v"(xa), "+v"(xb), "+v"(zo), "+v"(ho), "+v"(lb0), "+v"(lb1), "+v"(lb2[0]), "+v"(lb2[1]), "+v"(lb2[2]), "+v"(lb2[3]), "+v"(lb3));
    float cs0 = cs[0];
    asm volatile("" : "+v"(cs0), "+v"(aad[0]), "+v"(aad[1]), "+v"(aad[2]), "+v"(aad[3]));
    ST(6);
    u32x4 sel = {n16 == 0 ? 0x3f80u : n16 == 1 ? 0x3f800000u : 0u, n16 == 2 ? 0x3f80u : n16 == 3 ? 0x3f800000u : 0u, n16 == 4 ? 0x3f80u : n16 == 5 ? 0x3f800000u : 0u, n16 == 6 ? 0x3f80u : n16 == 7 ? 0x3f800000u : 0u};
    int yb = WS_Y + gq * 1024 + wave * 256 + n16 * 16;
    asm volatile("" : "+v"(sel), "+v"(yb));
    lds_spin_ge(lc + C_WLAND, 2u);
    read_down();
    u32x4 yv[4];
#pragma unroll 1
    for (int sp = 0; sp < SPIN_G; ++sp) {
      asm volatile("" ::: "memory");
#pragma unroll
      for (int k = 0; k < 4; ++k) yv[k] = __builtin_amdgcn_raw_buffer_load_b128(rs, yb, k * 4096, 16);
      unsigned mx = 0;
#pragma unroll
      for (int k = 0; k < 4; ++k) mx = umax3(umax3(mx, yv[k][0], yv[k][1]), yv[k][2], yv[k][3]);
      if (__all(mx != SENT)) break;
    }
    ST(7);
    CY(0);
    f32x4 ys = {0.f, 0.f, 0.f, 0.f};
#pragma unroll
    for (int k = 0; k < 4; ++k) ys = mfma(asb8(sel), asb8(yv[k]), ys);
    const u32x2 hp = {pk2(unpk(pk2(ys[0], ys[1])) + xa), pk2(unpk(pk2(ys[2], ys[3])) + xb)};
    *(LAS u32x2*)(L + lb0) = hp;
    CY(1);
    wgbar();
    CY(2);
    bf16x8 ha[4];
    ha[0] = *(LAS bf16x8*)(L + lb2[0]);
    const u32x4 h8 = *(LAS u32x4*)(L + lb1);
#pragma unroll
    for (int q = 1; q < 4; ++q) ha[q] = *(LAS bf16x8*)(L + lb2[q]);
    __builtin_amdgcn_sched_barrier(0);
    f32x4 y[4];
#pragma unroll
    for (int i = 0; i < 4; ++i) y[i] = f32x4{0.f, 0.f, 0.f, 0.f};
#pragma unroll
    for (int q = 0; q < 4; ++q)
#pragma unroll
      for (int i = 0; i < 4; ++i) y[i] = mfma(ha[q], wf[4 * q + i], y[i]);
    float sh = 0.f, sq = 0.f;
#pragma unroll
    for (int k = 0; k < 4; ++k) { sh = dot2(h8[k], 0x3f803f80u, sh); sq = dot2(h8[k], h8[k], sq); }
    auto sw = __builtin_amdgcn_permlane32_swap(__float_as_uint(sh), __float_as_uint(sq), false, false);
    const float r2 = row_sum(x16_sum(__uint_as_float(sw[0]) + __uint_as_float(sw[1]))) * (1.f / 512.f);
    sw = __builtin_amdgcn_permlane32_swap(__float_as_uint(r2), __float_as_uint(r2), false, false);
    const float mean = __uint_as_float(sw[0]);
    const float gu = fmaf(cs0, rbf(mean), x32_sum(x16_sum((y[0][0] + y[1][1]) + (y[2][2] + y[3][3]))));
    CY(3);
    *(LAS unsigned short*)(L + lb3) = (unsigned short)pk2(fmx(gu, 0.f) * dpp<ROR8>(gu), 0.f);
    __builtin_amdgcn_sched_barrier(0);
    CY(4);
    wgbar();
    bf16x8 ab[4];
#pragma unroll
    for (int q = 0; q < 4; ++q) ab[q] = *(LAS bf16x8*)(L + aad[q]);
    __builtin_amdgcn_sched_barrier(0);
    f32x4 z0 = {0.f, 0.f, 0.f, 0.f}, z1 = z0;
#pragma unroll
    for (int q = 0; q < 4; ++q) { z0 = mfma(ab[q], wf[16 + 2 * q], z0); z1 = mfma(ab[q], wf[17 + 2 * q], z1); }
    const float var = fmx(fmaf(-mean, mean, __uint_as_float(sw[1])), 0.f);
    const float rsd = __builtin_amdgcn_rcpf(rbf(__builtin_amdgcn_sqrtf(rbf(rbf(var) + 1e-5f))));
    BST64((u32x2{fns(z0[0] * (rsd * rsd)), fns(z1[0] * (rsd * rsd))}), zo);
    if (bi == 0) BST128((u32x4{hp[0] << 16, hp[0] & 0xffff0000u, hp[1] << 16, hp[1] & 0xffff0000u}), ho);
    CY(5); CYOUT(6);
    ST(8);
  }
  if (roleC && wave == 1) {
    const int zb = WS_Z + (lane >> 3) * 2048 + ci * 64 + (lane & 7) * 8, c0 = (ci >> 1) * 32 + (ci & 1) * 8 + (lane & 7);
    u32x2 zv[8];
#pragma unroll 1
    for (int sp = 0; sp < SPIN_G; ++sp) {
      asm volatile("" ::: "memory");
      zv[7] = u32x2{BLD32(WS_Z + 56 * 2048 + c0 * 4), BLD32(WS_Z + 56 * 2048 + c0 * 4 + 64)};
#pragma unroll
      for (int i = 0; i < 7; ++i) zv[i] = BLD64(zb + i * 16384);
      unsigned mx = 0;
#pragma unroll
      for (int i = 0; i < 8; ++i) mx = umax3(mx, zv[i][0], zv[i][1]);
      if (__all(mx != SENT)) break;
    }
    ST(7);
    const f32x2 t = ((asf2(zv[0]) + asf2(zv[1])) + (asf2(zv[2]) + asf2(zv[3]))) + ((asf2(zv[4]) + asf2(zv[5])) + asf2(zv[6]));
    float t0 = t[0], t1 = t[1];
    t0 += dpp<ROR8>(t0); t1 += dpp<ROR8>(t1);
    t0 = x32_sum(x16_sum(t0)); t1 = x32_sum(x16_sum(t1));
    const unsigned o = pk2(unpk(pk2(t0, t1)) + asf2(zv[7]));
    if (lane < 16) out[c0 + (lane & 8) * 2] = (bf16_t)(lane < 8 ? o : o >> 16);
    ST(8);
  }
  __builtin_amdgcn_s_waitcnt(0);
}

extern "C" void kernel_launch(void* const* d_in, const int* in_sizes, int n_in, void* d_out, int out_size, void* d_ws, size_t ws_size, hipStream_t stream) {
  fused_decode<<<NWG, NTHR, 0, stream>>>((const bf16_t*)d_in[0], (const bf16_t*)d_in[1], (const bf16_t*)d_in[2], (const bf16_t*)d_in[3], (const bf16_t*)d_in[4],
                                         (const bf16_t*)d_in[6], (const bf16_t*)d_in[7], (const bf16_t*)d_in[8], (bf16_t*)d_out, (char*)d_ws);
}
```
